```python
import torch

def _softmax(x, axis=-1):
    return torch.softmax(x, dim=axis)
def _gelu(x):
    return torch.nn.functional.gelu(x, approximate="tanh")
def _silu(x):
    return torch.nn.functional.silu(x)
def _sigmoid(x):
    return torch.sigmoid(x)
def _layernorm(x, axis=-1, eps=1e-5):
    m = x.mean(dim=axis, keepdim=True)
    v = x.var(dim=axis, keepdim=True, unbiased=False)
    return (x - m) / torch.sqrt(v + eps)
def _scatter_add(tgt, idx, src, axis=0):
    return tgt.clone().index_add_(axis, idx, src)
def _segment_sum(src, seg, n):
    out = torch.zeros((n,) + src.shape[1:], dtype=src.dtype)
    return out.index_add_(0, seg, src)
def _pad(x, pw):
    flat = tuple(v for pair in reversed(pw) for v in pair)
    return torch.nn.functional.pad(x, flat)

kernel_name = 'standalone__w4a16_gemm_dequant__prompt'

def setup_inputs():

    _w = (lambda a: (lambda t: t.to(torch.bfloat16) if t.is_floating_point() else t)(torch.as_tensor(a)))
    B, S, D, F = 1, 2048, 1024, 3584
    G = 32 if 32 > 0 else D
    D = (D // G) * G
    ng = D // G
    qw = torch.randint(0, 16, (D, F)).to(torch.int32)
    return {
        "x": _w((lambda *s: torch.randn(*s, dtype=torch.bfloat16) * 0.05)(B, S, D)),
        "qw": _w(qw),
        "scales": _w(torch.abs((lambda *s: torch.randn(*s, dtype=torch.bfloat16) * 0.05)(ng, F)) + 0.01),
        "zeros": _w(torch.full((ng, F), 8.0, dtype=float)),
    }

def reference(x, qw, scales, zeros):
    B, S, D = x.shape
    ng, F = scales.shape
    G = D // ng
    s = (lambda x, n, axis: torch.repeat_interleave(x, n, dim=axis))(scales, G, axis=0)
    z = (lambda x, n, axis: torch.repeat_interleave(x, n, dim=axis))(zeros, G, axis=0)
    w = ((lambda x, dt: x.to(dt))(qw, x.dtype) - z) * s
    return x @ w
```

```cpp
#include <hip/hip_runtime.h>
#include <hip/hip_bf16.h>

typedef __attribute__((ext_vector_type(8))) short bf16x8;
typedef __attribute__((ext_vector_type(4))) float f32x4;
typedef __attribute__((ext_vector_type(4))) int i32x4;
typedef __attribute__((ext_vector_type(2))) unsigned u32x2;

constexpr int M_ = 2048, K_ = 1024, N_ = 3584, G_ = 32;
constexpr int BM = 256, BN = 112, BK = 32;

__device__ __forceinline__ float bf2f(unsigned short v) { return __uint_as_float(((unsigned)v) << 16); }
__device__ __forceinline__ unsigned pk(float a, float b) {
  unsigned r; asm("v_cvt_pk_bf16_f32 %0, %1, %2" : "=v"(r) : "v"(a), "v"(b)); return r;
}

__global__ __launch_bounds__(256) void k0(const unsigned short* __restrict__ x, const int* __restrict__ qw,
                                          const unsigned short* __restrict__ sc, const unsigned short* __restrict__ zr,
                                          unsigned short* __restrict__ out) {
  __shared__ __attribute__((aligned(16))) unsigned short wl[7 * 64 * 8];
  int bid = blockIdx.x;
  int xcd = bid & 7, j = bid >> 3;
  int mb = j & 7, nb = xcd * 4 + (j >> 3);
  int tid = threadIdx.x, lane = tid & 63, wid = tid >> 6;
  int m0 = mb * BM + wid * 64, n0 = nb * BN;
  f32x4 acc[4][7] = {};
  for (int k0 = 0; k0 < K_; k0 += BK) {
    __syncthreads();
    if (tid < 112) {
      int oct = tid / 28, q = tid % 28;
      int n = n0 + q * 4;
      float s[4], zs[4];
      for (int c = 0; c < 4; ++c) { s[c] = bf2f(sc[(k0 / G_) * N_ + n + c]); zs[c] = -bf2f(zr[(k0 / G_) * N_ + n + c]) * s[c]; }
      i32x4 v[8];
      for (int r = 0; r < 8; ++r) v[r] = *(const i32x4*)(qw + (size_t)(k0 + oct * 8 + r) * N_ + n);
      for (int c = 0; c < 4; ++c) {
        unsigned p[4];
        for (int r = 0; r < 4; ++r) p[r] = pk(fmaf((float)v[2 * r][c], s[c], zs[c]), fmaf((float)v[2 * r + 1][c], s[c], zs[c]));
        int nn = q * 4 + c;
        int t = nn >> 4, rr = nn & 15;
        *(uint4*)(wl + (t * 64 + oct * 16 + rr) * 8) = make_uint4(p[0], p[1], p[2], p[3]);
      }
    }
    __syncthreads();
    bf16x8 xf[4];
    for (int m = 0; m < 4; ++m) xf[m] = *(const bf16x8*)(x + (size_t)(m0 + m * 16 + (lane & 15)) * K_ + k0 + (lane >> 4) * 8);
    for (int t = 0; t < 7; ++t) {
      bf16x8 wf = *(const bf16x8*)(wl + (t * 64 + lane) * 8);
      for (int m = 0; m < 4; ++m) acc[m][t] = __builtin_amdgcn_mfma_f32_16x16x32_bf16(wf, xf[m], acc[m][t], 0, 0, 0);
    }
  }
  for (int m = 0; m < 4; ++m)
    for (int t = 0; t < 7; ++t) {
      u32x2 o; o[0] = pk(acc[m][t][0], acc[m][t][1]); o[1] = pk(acc[m][t][2], acc[m][t][3]);
      *(u32x2*)(out + (size_t)(m0 + m * 16 + (lane & 15)) * N_ + n0 + t * 16 + (lane >> 4) * 4) = o;
    }
}

extern "C" void kernel_launch(void* const* d_in, const int* in_sizes, int n_in, void* d_out, int out_size, void* d_ws,
                              size_t ws_size, hipStream_t stream) {
  k0<<<256, 256, 0, stream>>>((const unsigned short*)d_in[0], (const int*)d_in[1], (const unsigned short*)d_in[2],
                              (const unsigned short*)d_in[3], (unsigned short*)d_out);
}
```

```cpp
#include <hip/hip_runtime.h>
#include <hip/hip_bf16.h>

typedef __attribute__((ext_vector_type(8))) short bf16x8;
typedef __attribute__((ext_vector_type(4))) float f32x4;
typedef __attribute__((ext_vector_type(4))) int i32x4;
typedef __attribute__((ext_vector_type(2))) unsigned u32x2;

constexpr int M_ = 2048, K_ = 1024, N_ = 3584, G_ = 32;
constexpr int BM = 256, BN = 112, BK = 32;

__device__ __forceinline__ float bf2f(unsigned short v) { return __uint_as_float(((unsigned)v) << 16); }
__device__ __forceinline__ unsigned pk(float a, float b) {
  unsigned r; asm("v_cvt_pk_bf16_f32 %0, %1, %2" : "=v"(r) : "v"(a), "v"(b)); return r;
}

__global__ __launch_bounds__(256) void k0(const unsigned short* __restrict__ x, const int* __restrict__ qw,
                                          const unsigned short* __restrict__ sc, const unsigned short* __restrict__ zr,
                                          unsigned short* __restrict__ out) {
  __shared__ __attribute__((aligned(16))) unsigned short wl[7 * 64 * 8];
  int bid = blockIdx.x;
  int xcd = bid & 7, j = bid >> 3;
  int mb = j & 7, nb = xcd * 4 + (j >> 3);
  int tid = threadIdx.x, lane = tid & 63, wid = tid >> 6;
  int m0 = mb * BM + wid * 64, n0 = nb * BN;
  f32x4 acc[4][7] = {};
  for (int k0 = 0; k0 < K_; k0 += BK) {
    __syncthreads();
    if (tid < 112) {
      int oct = tid / 28, q = tid % 28;
      int n = n0 + q * 4;
      float s[4], zs[4];
      for (int c = 0; c < 4; ++c) { s[c] = bf2f(sc[(k0 / G_) * N_ + n + c]); zs[c] = -bf2f(zr[(k0 / G_) * N_ + n + c]) * s[c]; }
      i32x4 v[8];
      for (int r = 0; r < 8; ++r) v[r] = *(const i32x4*)(qw + (size_t)(k0 + oct * 8 + r) * N_ + n);
      for (int c = 0; c < 4; ++c) {
        unsigned p[4];
        for (int r = 0; r < 4; ++r) p[r] = pk(fmaf((float)v[2 * r][c], s[c], zs[c]), fmaf((float)v[2 * r + 1][c], s[c], zs[c]));
        int nn = q * 4 + c;
        int t = nn >> 4, rr = nn & 15;
        *(uint4*)(wl + (t * 64 + oct * 16 + rr) * 8) = make_uint4(p[0], p[1], p[2], p[3]);
      }
    }
    __syncthreads();
    bf16x8 xf[4];
    for (int m = 0; m < 4; ++m) xf[m] = *(const bf16x8*)(x + (size_t)(m0 + m * 16 + (lane & 15)) * K_ + k0 + (lane >> 4) * 8);
    for (int t = 0; t < 7; ++t) {
      bf16x8 wf = *(const bf16x8*)(wl + (t * 64 + lane) * 8);
      for (int m = 0; m < 4; ++m) acc[m][t] = __builtin_amdgcn_mfma_f32_16x16x32_bf16(wf, xf[m], acc[m][t], 0, 0, 0);
    }
  }
  for (int m = 0; m < 4; ++m)
    for (int t = 0; t < 7; ++t) {
      u32x2 o; o[0] = pk(acc[m][t][0], acc[m][t][1]); o[1] = pk(acc[m][t][2], acc[m][t][3]);
      *(u32x2*)(out + (size_t)(m0 + m * 16 + (lane & 15)) * N_ + n0 + t * 16 + (lane >> 4) * 4) = o;
    }
}


__global__ void probe(const char* p, int stride, int n, int* sink) {
  unsigned off = 0; int v = 0;
  for (int i = 0; i < n; ++i) {
    asm volatile("global_load_dword %0, %1, %2\n\ts_waitcnt vmcnt(0)\n\tv_and_b32 %0, 0, %0" : "=&v"(v) : "v"(off), "s"(p) : "memory");
    off += stride + v;
  }
  if (v == 12345) sink[0] = v;
}

extern "C" void kernel_launch(void* const* d_in, const int* in_sizes, int n_in, void* d_out, int out_size, void* d_ws,
                              size_t ws_size, hipStream_t stream) {
  probe<<<1, 64, 0, stream>>>((const char*)d_in[1], 14336, 1, (int*)d_ws);
  k0<<<256, 256, 0, stream>>>((const unsigned short*)d_in[0], (const int*)d_in[1], (const unsigned short*)d_in[2],
                              (const unsigned short*)d_in[3], (unsigned short*)d_out);
}
```

```cpp
#include <hip/hip_runtime.h>

__global__ __launch_bounds__(256, 1) __attribute__((aligned(4096))) void w4a16_gemm(const char* x, const char* qw, const char* sc, const char* zr, char* out, char* ws) {
  asm volatile(
    "TOP_%=:\n"
    "s_load_dwordx8 s[4:11], %0, 0\n"
    "s_load_dwordx4 s[12:15], %0, 32\n"
    "s_getpc_b64 s[74:75]\n"
    "s_lshr_b32 s33, %1, 3\n"
    "s_mul_i32 s33, s33, 5\n"
    "v_min_u32 v23, 4, %2\n"
    "v_add_u32 v23, s33, v23\n"
    "v_min_u32 v23, (END_%=-TOP_%=)/128-1, v23\n"
    "v_lshlrev_b32 v23, 7, v23\n"
    "global_load_dword v23, v23, s[74:75]\n"
    "v_readfirstlane_b32 s18, %2\n"
    "s_and_b32 s33, %1, 3\n"
    "s_bfe_u32 s19, %1, 0x20003\n"
    "s_lshl2_add_u32 s19, s33, s19\n"
    "s_lshr_b32 s21, %1, 5\n"
    "s_lshr_b32 s18, s18, 6\n"
    "v_bfe_u32 v28, %2, 5, 1\n"
    "v_and_b32 v29, 31, %2\n"
    "v_xor_b32 v29, v29, v28\n"
    "v_lshlrev_b32 v29, 4, v29\n"
    "v_lshl_or_b32 v2, v28, 11, v29\n"
    "s_lshl_b32 s33, s19, 9\n"
    "s_add_u32 s33, s33, s18\n"
    "s_add_u32 s33, s33, 8\n"
    "s_lshl_b32 s33, s33, 9\n"
    "s_mul_i32 s65, s18, 33792\n"
    "s_add_u32 s65, s65, 18944\n"
    "v_add_u32 v3, 0x2000, v2\n"
    "v_add_u32 v4, 0x4000, v2\n"
    "v_add_u32 v5, 0x6000, v2\n"
    "s_waitcnt lgkmcnt(0)\n"
    "s_add_u32 s24, s4, s33\n"
    "s_addc_u32 s25, s5, 0\n"
    "s_xor_b32 s72, s18, s21\n"
    "s_lshl_b32 s72, s72, 15\n"
    "s_add_u32 s60, s24, s72\n"
    "s_addc_u32 s61, s25, 0\n"
    "s_add_u32 m0, s65, 4096\n"
    "s_nop 0\n"
    "global_load_lds_dwordx4 v2, s[60:61] offset:-4096 \n"
    "s_add_u32 m0, s65, 1056\n"
    "s_nop 0\n"
    "global_load_lds_dwordx4 v2, s[60:61] offset:0 \n"
    "s_add_u32 m0, s65, 6208\n"
    "s_nop 0\n"
    "global_load_lds_dwordx4 v3, s[60:61] offset:-4096 \n"
    "s_add_u32 m0, s65, 3168\n"
    "s_nop 0\n"
    "global_load_lds_dwordx4 v3, s[60:61] offset:0 \n"
    "s_bfe_u32 s72, %1, 0x10002\n"
    "s_lshl3_add_u32 s20, s72, s21\n"
    "s_lshr_b32 s26, s20, 1\n"
    "s_and_b32 s26, s26, 2\n"
    "s_lshl_b32 s26, s26, 12\n"
    "v_and_b32 v28, 63, %2\n"
    "v_cmp_gt_u32_e64 s[68:69], 4, v28\n"
    "v_lshlrev_b32 v1, 4, v28\n"
    "s_lshl_b32 s34, s18, 2\n"
    "v_add_u32 v29, s34, v28\n"
    "v_mov_b32 v30, s19\n"
    "v_cndmask_b32_e64 v29, v30, v29, s[68:69]\n"
    "v_max_u32 v28, 32, v28\n"
    "v_lshlrev_b32 v28, 2, v28\n"
    "v_lshl_add_u32 v28, v29, 7, v28\n"
    "s_lshl_b32 s34, s20, 20\n"
    "s_add_u32 s34, s34, 917376\n"
    "s_add_u32 s34, s34, s26\n"
    "v_add_u32 v18, s34, v28\n"
    "global_load_dword v19, v18, s[14:15] sc1\n"
    "s_bfe_u32 s33, %1, 0x10002\n"
    "s_lshl3_add_u32 s20, s33, s21\n"
    "s_mul_i32 s23, s20, 448\n"
    "s_add_u32 s22, s23, 64\n"
    "s_add_u32 s33, s23, 384\n"
    "s_bitcmp1_b32 s20, 0\n"
    "s_cselect_b32 s22, s22, s23\n"
    "s_cselect_b32 s23, s23, s33\n"
    "s_lshr_b32 s33, s18, 1\n"
    "s_lshl1_add_u32 s33, s19, s33\n"
    "s_mul_i32 s33, s33, 7168\n"
    "s_and_b32 s34, s18, 1\n"
    "s_lshl_b32 s34, s34, 7\n"
    "s_add_u32 s34, s34, s22\n"
    "v_and_b32 v28, 15, %2\n"
    "v_lshl_add_u32 v28, v28, 3, s34\n"
    "v_add_u32 v7, s33, v28\n"
    "s_lshl_b32 s35, s33, 6\n"
    "v_lshl_add_u32 v28, v28, 1, s35\n"
    "v_bfe_u32 v29, %2, 4, 2\n"
    "v_mul_u32_u24 v29, 114688, v29\n"
    "v_add_u32 v6, v28, v29\n"
    "s_mov_b64 s[36:37], s[6:7]\n"
    "s_add_u32 s38, s36, 14336\n"
    "s_addc_u32 s39, s37, 0\n"
    "s_add_u32 s40, s38, 14336\n"
    "s_addc_u32 s41, s39, 0\n"
    "s_add_u32 s42, s40, 14336\n"
    "s_addc_u32 s43, s41, 0\n"
    "s_add_u32 s44, s42, 14336\n"
    "s_addc_u32 s45, s43, 0\n"
    "s_add_u32 s46, s44, 14336\n"
    "s_addc_u32 s47, s45, 0\n"
    "s_add_u32 s48, s46, 14336\n"
    "s_addc_u32 s49, s47, 0\n"
    "s_add_u32 s50, s48, 14336\n"
    "s_addc_u32 s51, s49, 0\n"
    "global_load_dwordx2 v[24:25], v7, s[8:9]\n"
    "global_load_dwordx2 v[26:27], v7, s[10:11]\n"
    "global_load_dwordx4 v[192:195], v6, s[36:37] nt\n"
    "global_load_dwordx4 v[196:199], v6, s[38:39] nt\n"
    "global_load_dwordx4 v[200:203], v6, s[40:41] nt\n"
    "global_load_dwordx4 v[204:207], v6, s[42:43] nt\n"
    "global_load_dwordx4 v[208:211], v6, s[44:45] nt\n"
    "global_load_dwordx4 v[212:215], v6, s[46:47] nt\n"
    "global_load_dwordx4 v[216:219], v6, s[48:49] nt\n"
    "global_load_dwordx4 v[220:223], v6, s[50:51] nt\n"
    "s_add_u32 m0, s65, 8320\n"
    "s_nop 0\n"
    "global_load_lds_dwordx4 v4, s[60:61] offset:-4096 \n"
    "s_add_u32 m0, s65, 5280\n"
    "s_nop 0\n"
    "global_load_lds_dwordx4 v4, s[60:61] offset:0 \n"
    "s_add_u32 m0, s65, 10432\n"
    "s_nop 0\n"
    "global_load_lds_dwordx4 v5, s[60:61] offset:-4096 \n"
    "s_add_u32 m0, s65, 7392\n"
    "s_nop 0\n"
    "global_load_lds_dwordx4 v5, s[60:61] offset:0 \n"
    "s_lshr_b32 s26, s19, 2\n"
    "s_mul_i32 s26, s26, 1\n"
    "s_lshl2_add_u32 s27, s19, s26\n"
    "v_and_b32 v28, 63, %2\n"
    "v_cmp_gt_u32_e64 s[68:69], 32, v28\n"
    "v_and_b32 v28, 15, %2\n"
    "v_and_b32 v29, 7, %2\n"
    "v_cndmask_b32_e64 v28, v29, v28, s[68:69]\n"
    "v_bfe_u32 v29, %2, 4, 1\n"
    "v_bfe_u32 v30, %2, 3, 1\n"
    "v_cndmask_b32_e64 v29, v30, v29, s[68:69]\n"
    "s_add_u32 s34, s22, 256\n"
    "v_mov_b32 v30, s23\n"
    "v_mov_b32 v31, s34\n"
    "v_cndmask_b32_e64 v30, v30, v31, s[68:69]\n"
    "v_lshl_add_u32 v30, v28, 3, v30\n"
    "v_add_u32 v9, s33, v30\n"
    "s_lshl2_add_u32 s34, s19, s18\n"
    "s_mul_i32 s34, s34, 229376\n"
    "v_lshl_add_u32 v30, v30, 1, s34\n"
    "v_mul_u32_u24 v31, 114688, v29\n"
    "v_add_u32 v8, v30, v31\n"
    "s_lshl_b32 s35, s20, 20\n"
    "s_lshl1_add_u32 s34, s27, s18\n"
    "s_lshl_b32 s34, s34, 9\n"
    "s_add_u32 s34, s34, s35\n"
    "v_lshl_add_u32 v28, v29, 4, v28\n"
    "v_lshl_add_u32 v28, v28, 4, s34\n"
    "v_mov_b32 v29, 786432\n"
    "v_mov_b32 v30, 524288\n"
    "v_cndmask_b32_e64 v29, v29, v30, s[68:69]\n"
    "v_add_u32 v10, v28, v29\n"
    "v_mov_b32 v29, 128\n"
    "v_mov_b32 v30, 131072\n"
    "v_cndmask_b32_e64 v29, v29, v30, s[68:69]\n"
    "v_add_u32 v11, v10, v29\n"
    "s_lshr_b32 s33, s18, 1\n"
    "s_add_u32 s33, s27, s33\n"
    "s_lshl_b32 s33, s33, 10\n"
    "s_add_u32 s33, s33, s35\n"
    "s_and_b32 s34, s18, 1\n"
    "s_lshl_b32 s34, s34, 18\n"
    "s_add_u32 s33, s33, s34\n"
    "s_add_u32 s28, s14, s33\n"
    "s_addc_u32 s29, s15, 0\n"
    "s_add_u32 s30, s28, 131072\n"
    "s_addc_u32 s31, s29, 0\n"
    "s_mul_i32 s33, s18, 17408\n"
    "s_add_u32 s33, s33, 4096\n"
    "s_add_u32 s33, s33, s35\n"
    "v_add_u32 v248, s33, v1\n"
    "v_and_b32 v28, 1, %2\n"
    "v_bfe_u32 v29, %2, 4, 2\n"
    "v_xor_b32 v30, v28, v29\n"
    "v_lshl_add_u32 v30, v28, 5, v30\n"
    "v_lshl_add_u32 v30, v30, 4, s65\n"
    "v_bfe_u32 v31, %2, 1, 3\n"
    "v_mul_u32_u24 v31, 1056, v31\n"
    "v_add_u32 v12, v30, v31\n"
    "s_mul_i32 s64, s18, 4736\n"
    "s_add_u32 s33, s18, 3\n"
    "s_and_b32 s33, s33, 3\n"
    "s_mul_i32 s33, s33, 4736\n"
    "v_mul_u32_u24 v28, 288, v28\n"
    "v_and_b32 v30, 2, %2\n"
    "v_mul_u32_u24 v30, 320, v30\n"
    "v_add3_u32 v28, v28, v30, s33\n"
    "v_and_b32 v30, 12, %2\n"
    "v_add_u32 v30, v30, v29\n"
    "v_lshl_add_u32 v13, v30, 4, v28\n"
    "s_add_u32 s33, s18, 1\n"
    "s_and_b32 s33, s33, 3\n"
    "s_xor_b32 s33, s33, s21\n"
    "s_lshl3_add_u32 s33, s19, s33\n"
    "s_xor_b32 s34, s33, 4\n"
    "s_mul_i32 s33, s33, 114688\n"
    "s_mul_i32 s34, s34, 114688\n"
    "v_mul_u32_u24 v29, 28672, v29\n"
    "v_and_b32 v28, 15, %2\n"
    "v_lshl_add_u32 v28, v28, 3, v29\n"
    "v_add_u32 v28, s22, v28\n"
    "v_add_u32 v14, s33, v28\n"
    "v_add_u32 v15, s34, v28\n"
    "v_and_b32 v28, 7, %2\n"
    "v_lshl_add_u32 v28, v28, 3, v29\n"
    "v_bfe_u32 v29, %2, 3, 1\n"
    "v_lshl_add_u32 v28, v29, 2, v28\n"
    "v_add_u32 v28, s23, v28\n"
    "v_add_u32 v16, s33, v28\n"
    "v_add_u32 v17, s34, v28\n"
    "s_mov_b64 s[52:53], s[12:13]\n"
    "s_add_u32 s54, s52, 7168\n"
    "s_addc_u32 s55, s53, 0\n"
    "s_add_u32 s56, s54, 7168\n"
    "s_addc_u32 s57, s55, 0\n"
    "s_add_u32 s58, s56, 7168\n"
    "s_addc_u32 s59, s57, 0\n"
    "s_cmp_eq_u32 s18, 0\n"
    "s_cselect_b32 s67, -1, 0\n"
    "s_mov_b32 s66, 0\n"
    "s_mov_b32 s73, 0\n"
    "s_mov_b32 s62, -1\n"
    "s_mov_b32 s63, 0xffff\n"
    "s_and_b32 s72, s19, 1\n"
    "s_lshl_b32 s72, s72, 11\n"
    "v_or_b32 v160, s72, v1\n"
    "v_xor_b32 v161, 0x800, v160\n"
    "s_waitcnt vmcnt(19)\n"
    "s_mov_b64 vcc, -1\n"
    "s_branch HOP0_%=\n"
    "HOPDONE_%=:\n"
    "s_mov_b64 vcc, 0\n"
    "s_xor_b32 s72, s18, s21\n"
    "s_xor_b32 s72, s72, 4\n"
    "s_lshl_b32 s72, s72, 15\n"
    "s_add_u32 s60, s24, s72\n"
    "s_addc_u32 s61, s25, 0\n"
    "s_add_u32 m0, s65, 12544\n"
    "s_nop 0\n"
    "global_load_lds_dwordx4 v2, s[60:61] offset:-4096 \n"
    "s_add_u32 m0, s65, 9504\n"
    "s_nop 0\n"
    "global_load_lds_dwordx4 v2, s[60:61] offset:0 \n"
    "s_add_u32 m0, s65, 14656\n"
    "s_nop 0\n"
    "global_load_lds_dwordx4 v3, s[60:61] offset:-4096 \n"
    "s_add_u32 m0, s65, 11616\n"
    "s_nop 0\n"
    "global_load_lds_dwordx4 v3, s[60:61] offset:0 \n"
    "s_add_u32 m0, s65, 16768\n"
    "s_nop 0\n"
    "global_load_lds_dwordx4 v4, s[60:61] offset:-4096 \n"
    "s_add_u32 m0, s65, 13728\n"
    "s_nop 0\n"
    "global_load_lds_dwordx4 v4, s[60:61] offset:0 \n"
    "s_add_u32 m0, s65, 18880\n"
    "s_nop 0\n"
    "global_load_lds_dwordx4 v5, s[60:61] offset:-4096 \n"
    "s_add_u32 m0, s65, 15840\n"
    "s_nop 0\n"
    "global_load_lds_dwordx4 v5, s[60:61] offset:0 \n"
    "s_waitcnt vmcnt(20)\n"
    "v_lshlrev_b32 v240, 16, v24\n"
    "v_and_b32 v241, 0xffff0000, v24\n"
    "v_lshlrev_b32 v242, 16, v25\n"
    "v_and_b32 v243, 0xffff0000, v25\n"
    "v_lshlrev_b32 v244, 16, v26\n"
    "v_and_b32 v245, 0xffff0000, v26\n"
    "v_lshlrev_b32 v246, 16, v27\n"
    "v_and_b32 v247, 0xffff0000, v27\n"
    "v_mul_f32_e64 v244, -v244, v240\n"
    "v_mul_f32_e64 v245, -v245, v241\n"
    "v_mul_f32_e64 v246, -v246, v242\n"
    "v_mul_f32_e64 v247, -v247, v243\n"
    "s_waitcnt vmcnt(18)\n"
    "v_cvt_f32_i32 v192, v192\n"
    "v_cvt_f32_i32 v193, v193\n"
    "v_cvt_f32_i32 v194, v194\n"
    "v_cvt_f32_i32 v195, v195\n"
    "v_cvt_f32_i32 v196, v196\n"
    "v_cvt_f32_i32 v197, v197\n"
    "v_cvt_f32_i32 v198, v198\n"
    "v_cvt_f32_i32 v199, v199\n"
    "v_pk_fma_f32 v[192:193], v[192:193], v[240:241], v[244:245]\n"
    "v_pk_fma_f32 v[194:195], v[194:195], v[242:243], v[246:247]\n"
    "v_pk_fma_f32 v[196:197], v[196:197], v[240:241], v[244:245]\n"
    "v_pk_fma_f32 v[198:199], v[198:199], v[242:243], v[246:247]\n"
    "v_cvt_pk_bf16_f32 v224, v192, v196\n"
    "v_cvt_pk_bf16_f32 v228, v193, v197\n"
    "v_cvt_pk_bf16_f32 v232, v194, v198\n"
    "v_cvt_pk_bf16_f32 v236, v195, v199\n"
    "s_waitcnt vmcnt(16)\n"
    "v_cvt_f32_i32 v200, v200\n"
    "v_cvt_f32_i32 v201, v201\n"
    "v_cvt_f32_i32 v202, v202\n"
    "v_cvt_f32_i32 v203, v203\n"
    "v_cvt_f32_i32 v204, v204\n"
    "v_cvt_f32_i32 v205, v205\n"
    "v_cvt_f32_i32 v206, v206\n"
    "v_cvt_f32_i32 v207, v207\n"
    "v_pk_fma_f32 v[200:201], v[200:201], v[240:241], v[244:245]\n"
    "v_pk_fma_f32 v[202:203], v[202:203], v[242:243], v[246:247]\n"
    "v_pk_fma_f32 v[204:205], v[204:205], v[240:241], v[244:245]\n"
    "v_pk_fma_f32 v[206:207], v[206:207], v[242:243], v[246:247]\n"
    "v_cvt_pk_bf16_f32 v225, v200, v204\n"
    "v_cvt_pk_bf16_f32 v229, v201, v205\n"
    "v_cvt_pk_bf16_f32 v233, v202, v206\n"
    "v_cvt_pk_bf16_f32 v237, v203, v207\n"
    "s_waitcnt vmcnt(14)\n"
    "v_cvt_f32_i32 v208, v208\n"
    "v_cvt_f32_i32 v209, v209\n"
    "v_cvt_f32_i32 v210, v210\n"
    "v_cvt_f32_i32 v211, v211\n"
    "v_cvt_f32_i32 v212, v212\n"
    "v_cvt_f32_i32 v213, v213\n"
    "v_cvt_f32_i32 v214, v214\n"
    "v_cvt_f32_i32 v215, v215\n"
    "v_pk_fma_f32 v[208:209], v[208:209], v[240:241], v[244:245]\n"
    "v_pk_fma_f32 v[210:211], v[210:211], v[242:243], v[246:247]\n"
    "v_pk_fma_f32 v[212:213], v[212:213], v[240:241], v[244:245]\n"
    "v_pk_fma_f32 v[214:215], v[214:215], v[242:243], v[246:247]\n"
    "v_cvt_pk_bf16_f32 v226, v208, v212\n"
    "v_cvt_pk_bf16_f32 v230, v209, v213\n"
    "v_cvt_pk_bf16_f32 v234, v210, v214\n"
    "v_cvt_pk_bf16_f32 v238, v211, v215\n"
    "s_waitcnt vmcnt(12)\n"
    "v_cvt_f32_i32 v216, v216\n"
    "v_cvt_f32_i32 v217, v217\n"
    "v_cvt_f32_i32 v218, v218\n"
    "v_cvt_f32_i32 v219, v219\n"
    "v_cvt_f32_i32 v220, v220\n"
    "v_cvt_f32_i32 v221, v221\n"
    "v_cvt_f32_i32 v222, v222\n"
    "v_cvt_f32_i32 v223, v223\n"
    "v_pk_fma_f32 v[216:217], v[216:217], v[240:241], v[244:245]\n"
    "v_pk_fma_f32 v[218:219], v[218:219], v[242:243], v[246:247]\n"
    "v_pk_fma_f32 v[220:221], v[220:221], v[240:241], v[244:245]\n"
    "v_pk_fma_f32 v[222:223], v[222:223], v[242:243], v[246:247]\n"
    "v_cvt_pk_bf16_f32 v227, v216, v220\n"
    "v_cvt_pk_bf16_f32 v231, v217, v221\n"
    "v_cvt_pk_bf16_f32 v235, v218, v222\n"
    "v_cvt_pk_bf16_f32 v239, v219, v223\n"
    "s_sub_u32 s72, s18, 1\n"
    "s_and_b32 s72, s72, 3\n"
    "s_xor_b32 s72, s72, s21\n"
    "s_lshl_b32 s72, s72, 15\n"
    "s_add_u32 s60, s24, s72\n"
    "s_addc_u32 s61, s25, 0\n"
    "s_add_u32 m0, s65, 20992\n"
    "s_nop 0\n"
    "global_load_lds_dwordx4 v2, s[60:61] offset:-4096 \n"
    "s_add_u32 m0, s65, 17952\n"
    "s_nop 0\n"
    "global_load_lds_dwordx4 v2, s[60:61] offset:0 \n"
    "s_add_u32 m0, s65, 23104\n"
    "s_nop 0\n"
    "global_load_lds_dwordx4 v3, s[60:61] offset:-4096 \n"
    "s_add_u32 m0, s65, 20064\n"
    "s_nop 0\n"
    "global_load_lds_dwordx4 v3, s[60:61] offset:0 \n"
    "s_waitcnt vmcnt(4)\n"
    "global_store_dwordx4 v160, v[224:227], s[28:29] sc1\n"
    "global_store_dwordx4 v161, v[228:231], s[28:29] sc1\n"
    "global_store_dwordx4 v160, v[232:235], s[30:31] sc1\n"
    "global_store_dwordx4 v161, v[236:239], s[30:31] sc1\n"
    "s_add_u32 m0, s65, 25216\n"
    "s_nop 0\n"
    "global_load_lds_dwordx4 v4, s[60:61] offset:-4096 \n"
    "s_add_u32 m0, s65, 22176\n"
    "s_nop 0\n"
    "global_load_lds_dwordx4 v4, s[60:61] offset:0 \n"
    "s_add_u32 m0, s65, 27328\n"
    "s_nop 0\n"
    "global_load_lds_dwordx4 v5, s[60:61] offset:-4096 \n"
    "s_add_u32 m0, s65, 24288\n"
    "s_nop 0\n"
    "global_load_lds_dwordx4 v5, s[60:61] offset:0 \n"
    "s_sub_u32 s72, s18, 1\n"
    "s_and_b32 s72, s72, 3\n"
    "s_xor_b32 s72, s72, s21\n"
    "s_xor_b32 s72, s72, 4\n"
    "s_lshl_b32 s72, s72, 15\n"
    "s_add_u32 s60, s24, s72\n"
    "s_addc_u32 s61, s25, 0\n"
    "s_add_u32 m0, s65, 29440\n"
    "s_nop 0\n"
    "global_load_lds_dwordx4 v2, s[60:61] offset:-4096 \n"
    "s_add_u32 m0, s65, 26400\n"
    "s_nop 0\n"
    "global_load_lds_dwordx4 v2, s[60:61] offset:0 \n"
    "s_add_u32 m0, s65, 31552\n"
    "s_nop 0\n"
    "global_load_lds_dwordx4 v3, s[60:61] offset:-4096 \n"
    "s_add_u32 m0, s65, 28512\n"
    "s_nop 0\n"
    "global_load_lds_dwordx4 v3, s[60:61] offset:0 \n"
    "s_add_u32 m0, s65, 33664\n"
    "s_nop 0\n"
    "global_load_lds_dwordx4 v4, s[60:61] offset:-4096 \n"
    "s_add_u32 m0, s65, 30624\n"
    "s_nop 0\n"
    "global_load_lds_dwordx4 v4, s[60:61] offset:0 \n"
    "s_add_u32 m0, s65, 35776\n"
    "s_nop 0\n"
    "global_load_lds_dwordx4 v5, s[60:61] offset:-4096 \n"
    "s_add_u32 m0, s65, 32736\n"
    "s_nop 0\n"
    "global_load_lds_dwordx4 v5, s[60:61] offset:0 \n"
    "ds_read_b128 a[0:3], v12 offset:0\n"
    "ds_read_b128 a[4:7], v12 offset:64\n"
    "ds_read_b128 a[8:11], v12 offset:128\n"
    "ds_read_b128 a[12:15], v12 offset:192\n"
    "ds_read_b128 a[16:19], v12 offset:256\n"
    "ds_read_b128 a[20:23], v12 offset:320\n"
    "ds_read_b128 a[24:27], v12 offset:384\n"
    "ds_read_b128 a[28:31], v12 offset:448\n"
    "v_and_b32 v21, -16, v19\n"
    "v_add_u32 v21, 16, v21\n"
    "s_waitcnt vmcnt(12)\n"
    "s_barrier\n"
    "v_or_b32 v22, 1, v21\n"
    "s_mov_b64 exec, s[66:67]\n"
    "global_store_dword v18, v22, s[14:15] sc1\n"
    "s_mov_b64 exec, -1\n"
    "s_waitcnt lgkmcnt(0)\n"
    "s_sub_u32 s72, s18, 2\n"
    "s_and_b32 s72, s72, 3\n"
    "s_xor_b32 s72, s72, s21\n"
    "s_lshl_b32 s72, s72, 15\n"
    "s_add_u32 s60, s24, s72\n"
    "s_addc_u32 s61, s25, 0\n"
    "s_add_u32 m0, s65, 4096\n"
    "s_nop 0\n"
    "global_load_lds_dwordx4 v2, s[60:61] offset:-4096 \n"
    "s_add_u32 m0, s65, 1056\n"
    "s_nop 0\n"
    "global_load_lds_dwordx4 v2, s[60:61] offset:0 \n"
    "s_add_u32 m0, s65, 6208\n"
    "s_nop 0\n"
    "global_load_lds_dwordx4 v3, s[60:61] offset:-4096 \n"
    "s_add_u32 m0, s65, 3168\n"
    "s_nop 0\n"
    "global_load_lds_dwordx4 v3, s[60:61] offset:0 \n"
    "s_add_u32 m0, s65, 8320\n"
    "s_nop 0\n"
    "global_load_lds_dwordx4 v4, s[60:61] offset:-4096 \n"
    "s_add_u32 m0, s65, 5280\n"
    "s_nop 0\n"
    "global_load_lds_dwordx4 v4, s[60:61] offset:0 \n"
    "s_add_u32 m0, s65, 10432\n"
    "s_nop 0\n"
    "global_load_lds_dwordx4 v5, s[60:61] offset:-4096 \n"
    "s_add_u32 m0, s65, 7392\n"
    "s_nop 0\n"
    "global_load_lds_dwordx4 v5, s[60:61] offset:0 \n"
    "ds_read_b128 a[32:35], v12 offset:8448\n"
    "ds_read_b128 a[36:39], v12 offset:8512\n"
    "ds_read_b128 a[40:43], v12 offset:8576\n"
    "ds_read_b128 a[44:47], v12 offset:8640\n"
    "ds_read_b128 a[48:51], v12 offset:8704\n"
    "ds_read_b128 a[52:55], v12 offset:8768\n"
    "ds_read_b128 a[56:59], v12 offset:8832\n"
    "ds_read_b128 a[60:63], v12 offset:8896\n"
    "s_waitcnt lgkmcnt(0)\n"
    "s_sub_u32 s72, s18, 2\n"
    "s_and_b32 s72, s72, 3\n"
    "s_xor_b32 s72, s72, s21\n"
    "s_xor_b32 s72, s72, 4\n"
    "s_lshl_b32 s72, s72, 15\n"
    "s_add_u32 s60, s24, s72\n"
    "s_addc_u32 s61, s25, 0\n"
    "s_add_u32 m0, s65, 12544\n"
    "s_nop 0\n"
    "global_load_lds_dwordx4 v2, s[60:61] offset:-4096 \n"
    "s_add_u32 m0, s65, 9504\n"
    "s_nop 0\n"
    "global_load_lds_dwordx4 v2, s[60:61] offset:0 \n"
    "s_add_u32 m0, s65, 14656\n"
    "s_nop 0\n"
    "global_load_lds_dwordx4 v3, s[60:61] offset:-4096 \n"
    "s_add_u32 m0, s65, 11616\n"
    "s_nop 0\n"
    "global_load_lds_dwordx4 v3, s[60:61] offset:0 \n"
    "s_add_u32 m0, s65, 16768\n"
    "s_nop 0\n"
    "global_load_lds_dwordx4 v4, s[60:61] offset:-4096 \n"
    "s_add_u32 m0, s65, 13728\n"
    "s_nop 0\n"
    "global_load_lds_dwordx4 v4, s[60:61] offset:0 \n"
    "s_add_u32 m0, s65, 18880\n"
    "s_nop 0\n"
    "global_load_lds_dwordx4 v5, s[60:61] offset:-4096 \n"
    "s_add_u32 m0, s65, 15840\n"
    "s_nop 0\n"
    "global_load_lds_dwordx4 v5, s[60:61] offset:0 \n"
    "s_waitcnt vmcnt(24)\n"
    "ds_read_b128 a[64:67], v12 offset:16896\n"
    "ds_read_b128 a[68:71], v12 offset:16960\n"
    "ds_read_b128 a[72:75], v12 offset:17024\n"
    "ds_read_b128 a[76:79], v12 offset:17088\n"
    "ds_read_b128 a[80:83], v12 offset:17152\n"
    "ds_read_b128 a[84:87], v12 offset:17216\n"
    "ds_read_b128 a[88:91], v12 offset:17280\n"
    "ds_read_b128 a[92:95], v12 offset:17344\n"
    "s_waitcnt lgkmcnt(0)\n"
    "s_sub_u32 s72, s18, 3\n"
    "s_and_b32 s72, s72, 3\n"
    "s_xor_b32 s72, s72, s21\n"
    "s_lshl_b32 s72, s72, 15\n"
    "s_add_u32 s60, s24, s72\n"
    "s_addc_u32 s61, s25, 0\n"
    "s_add_u32 m0, s65, 20992\n"
    "s_nop 0\n"
    "global_load_lds_dwordx4 v2, s[60:61] offset:-4096 \n"
    "s_add_u32 m0, s65, 17952\n"
    "s_nop 0\n"
    "global_load_lds_dwordx4 v2, s[60:61] offset:0 \n"
    "s_add_u32 m0, s65, 23104\n"
    "s_nop 0\n"
    "global_load_lds_dwordx4 v3, s[60:61] offset:-4096 \n"
    "s_add_u32 m0, s65, 20064\n"
    "s_nop 0\n"
    "global_load_lds_dwordx4 v3, s[60:61] offset:0 \n"
    "s_add_u32 m0, s65, 25216\n"
    "s_nop 0\n"
    "global_load_lds_dwordx4 v4, s[60:61] offset:-4096 \n"
    "s_add_u32 m0, s65, 22176\n"
    "s_nop 0\n"
    "global_load_lds_dwordx4 v4, s[60:61] offset:0 \n"
    "s_add_u32 m0, s65, 27328\n"
    "s_nop 0\n"
    "global_load_lds_dwordx4 v5, s[60:61] offset:-4096 \n"
    "s_add_u32 m0, s65, 24288\n"
    "s_nop 0\n"
    "global_load_lds_dwordx4 v5, s[60:61] offset:0 \n"
    "s_barrier\n"
    "global_load_dword v20, v18, s[14:15] sc1\n"
    "s_waitcnt vmcnt(25)\n"
    "ds_read_b128 a[96:99], v12 offset:25344\n"
    "ds_read_b128 a[100:103], v12 offset:25408\n"
    "ds_read_b128 a[104:107], v12 offset:25472\n"
    "ds_read_b128 a[108:111], v12 offset:25536\n"
    "ds_read_b128 a[112:115], v12 offset:25600\n"
    "ds_read_b128 a[116:119], v12 offset:25664\n"
    "ds_read_b128 a[120:123], v12 offset:25728\n"
    "ds_read_b128 a[124:127], v12 offset:25792\n"
    "s_waitcnt lgkmcnt(0)\n"
    "s_sub_u32 s72, s18, 3\n"
    "s_and_b32 s72, s72, 3\n"
    "s_xor_b32 s72, s72, s21\n"
    "s_xor_b32 s72, s72, 4\n"
    "s_lshl_b32 s72, s72, 15\n"
    "s_add_u32 s60, s24, s72\n"
    "s_addc_u32 s61, s25, 0\n"
    "s_add_u32 m0, s65, 29440\n"
    "s_nop 0\n"
    "global_load_lds_dwordx4 v2, s[60:61] offset:-4096 \n"
    "s_add_u32 m0, s65, 26400\n"
    "s_nop 0\n"
    "global_load_lds_dwordx4 v2, s[60:61] offset:0 \n"
    "s_add_u32 m0, s65, 31552\n"
    "s_nop 0\n"
    "global_load_lds_dwordx4 v3, s[60:61] offset:-4096 \n"
    "s_add_u32 m0, s65, 28512\n"
    "s_nop 0\n"
    "global_load_lds_dwordx4 v3, s[60:61] offset:0 \n"
    "s_add_u32 m0, s65, 33664\n"
    "s_nop 0\n"
    "global_load_lds_dwordx4 v4, s[60:61] offset:-4096 \n"
    "s_add_u32 m0, s65, 30624\n"
    "s_nop 0\n"
    "global_load_lds_dwordx4 v4, s[60:61] offset:0 \n"
    "s_add_u32 m0, s65, 35776\n"
    "s_nop 0\n"
    "global_load_lds_dwordx4 v5, s[60:61] offset:-4096 \n"
    "s_add_u32 m0, s65, 32736\n"
    "s_nop 0\n"
    "global_load_lds_dwordx4 v5, s[60:61] offset:0 \n"
    "s_waitcnt vmcnt(25)\n"
    "ds_read_b128 a[128:131], v12 offset:0\n"
    "ds_read_b128 a[132:135], v12 offset:64\n"
    "ds_read_b128 a[136:139], v12 offset:128\n"
    "ds_read_b128 a[140:143], v12 offset:192\n"
    "ds_read_b128 a[144:147], v12 offset:256\n"
    "ds_read_b128 a[148:151], v12 offset:320\n"
    "ds_read_b128 a[152:155], v12 offset:384\n"
    "ds_read_b128 a[156:159], v12 offset:448\n"
    "s_waitcnt vmcnt(8)\n"
    "v_cmp_eq_u32_e64 s[68:69], v20, v19\n"
    "v_and_b32 v22, 15, v20\n"
    "v_cmp_gt_u32_e64 s[70:71], 1, v22\n"
    "s_or_b32 s68, s68, s70\n"
    "s_and_b32 s68, s68, 15\n"
    "s_cbranch_scc1 POLL1_%=\n"
    "BACK1_%=:\n"
    "global_load_dwordx4 v[32:35], v248, s[14:15] offset:-4096 sc1\n"
    "v_add_u32 v250, 8192, v248\n"
    "global_load_dwordx4 v[36:39], v248, s[14:15] offset:-3072 sc1\n"
    "global_load_dwordx4 v[40:43], v248, s[14:15] offset:2048 sc1\n"
    "global_load_dwordx4 v[44:47], v248, s[14:15] offset:3072 sc1\n"
    "global_load_dwordx4 v[48:51], v250, s[14:15] offset:-4096 sc1\n"
    "global_load_dwordx4 v[52:55], v250, s[14:15] offset:-3072 sc1\n"
    "global_load_dwordx4 v[56:59], v250, s[14:15] offset:2048 sc1\n"
    "global_load_dwordx4 v[60:63], v250, s[14:15] offset:3072 sc1\n"
    "ds_read_b128 a[160:163], v12 offset:8448\n"
    "ds_read_b128 a[164:167], v12 offset:8512\n"
    "ds_read_b128 a[168:171], v12 offset:8576\n"
    "ds_read_b128 a[172:175], v12 offset:8640\n"
    "ds_read_b128 a[176:179], v12 offset:8704\n"
    "ds_read_b128 a[180:183], v12 offset:8768\n"
    "ds_read_b128 a[184:187], v12 offset:8832\n"
    "ds_read_b128 a[188:191], v12 offset:8896\n"
    "ds_read_b128 a[192:195], v12 offset:16896\n"
    "ds_read_b128 a[196:199], v12 offset:16960\n"
    "ds_read_b128 a[200:203], v12 offset:17024\n"
    "ds_read_b128 a[204:207], v12 offset:17088\n"
    "ds_read_b128 a[208:211], v12 offset:17152\n"
    "ds_read_b128 a[212:215], v12 offset:17216\n"
    "ds_read_b128 a[216:219], v12 offset:17280\n"
    "ds_read_b128 a[220:223], v12 offset:17344\n"
    "s_waitcnt vmcnt(8)\n"
    "ds_read_b128 a[224:227], v12 offset:25344\n"
    "ds_read_b128 a[228:231], v12 offset:25408\n"
    "ds_read_b128 a[232:235], v12 offset:25472\n"
    "ds_read_b128 a[236:239], v12 offset:25536\n"
    "ds_read_b128 a[240:243], v12 offset:25600\n"
    "ds_read_b128 a[244:247], v12 offset:25664\n"
    "ds_read_b128 a[248:251], v12 offset:25728\n"
    "ds_read_b128 a[252:255], v12 offset:25792\n"
    "global_load_dwordx4 v[64:67], v248, s[14:15] offset:-2048 sc1\n"
    "global_load_dwordx4 v[68:71], v248, s[14:15] offset:-1024 sc1\n"
    "global_load_dwordx4 v[72:75], v248, s[14:15] offset:0 sc1\n"
    "global_load_dwordx4 v[76:79], v248, s[14:15] offset:1024 sc1\n"
    "global_load_dwordx4 v[80:83], v250, s[14:15] offset:-2048 sc1\n"
    "global_load_dwordx4 v[84:87], v250, s[14:15] offset:-1024 sc1\n"
    "global_load_dwordx4 v[88:91], v250, s[14:15] offset:0 sc1\n"
    "global_load_dwordx4 v[92:95], v250, s[14:15] offset:1024 sc1\n"
    "s_mov_b32 m0, s64\n"
    "s_nop 0\n"
    "s_waitcnt lgkmcnt(0)\n"
    "s_branch BODY_%=\n"
    ".p2align 12\n"
    "BODY_%=:\n"
    "s_waitcnt vmcnt(15)\n"
    "HOP0_%=:\n"
    "s_cbranch_vccnz HOP1_%=\n"
    "v_mfma_f32_16x16x32_bf16 v[160:163], v[32:35], a[0:3], 0\n"
    "s_waitcnt vmcnt(14)\n"
    "v_mfma_f32_16x16x32_bf16 v[160:163], v[36:39], a[4:7], v[160:163]\n"
    "s_waitcnt vmcnt(13)\n"
    "v_mfma_f32_16x16x32_bf16 v[160:163], v[40:43], a[8:11], v[160:163]\n"
    "s_waitcnt vmcnt(12)\n"
    "v_mfma_f32_16x16x32_bf16 v[160:163], v[44:47], a[12:15], v[160:163]\n"
    "s_waitcnt vmcnt(11)\n"
    "v_mfma_f32_16x16x32_bf16 v[160:163], v[48:51], a[16:19], v[160:163]\n"
    "s_waitcnt vmcnt(10)\n"
    "v_mfma_f32_16x16x32_bf16 v[160:163], v[52:55], a[20:23], v[160:163]\n"
    "v_add_u32 v248, 131072, v248\n"
    "s_waitcnt vmcnt(9)\n"
    "v_mfma_f32_16x16x32_bf16 v[160:163], v[56:59], a[24:27], v[160:163]\n"
    "s_waitcnt vmcnt(8)\n"
    "v_mfma_f32_16x16x32_bf16 v[160:163], v[60:63], a[28:31], v[160:163]\n"
    "v_mfma_f32_16x16x32_bf16 v[164:167], v[32:35], a[32:35], 0\n"
    "v_mfma_f32_16x16x32_bf16 v[164:167], v[36:39], a[36:39], v[164:167]\n"
    "global_load_dwordx4 v[96:99], v248, s[14:15] offset:-4096 sc1\n"
    "v_add_u32 v250, 8192, v248\n"
    "v_mfma_f32_16x16x32_bf16 v[164:167], v[40:43], a[40:43], v[164:167]\n"
    "v_mfma_f32_16x16x32_bf16 v[164:167], v[44:47], a[44:47], v[164:167]\n"
    "v_mfma_f32_16x16x32_bf16 v[164:167], v[48:51], a[48:51], v[164:167]\n"
    "ds_write_addtid_b32 v160 offset:0\n"
    "v_mfma_f32_16x16x32_bf16 v[164:167], v[52:55], a[52:55], v[164:167]\n"
    "ds_write_addtid_b32 v161 offset:288\n"
    "global_load_dwordx4 v[100:103], v248, s[14:15] offset:-3072 sc1\n"
    "v_mfma_f32_16x16x32_bf16 v[164:167], v[56:59], a[56:59], v[164:167]\n"
    "ds_write_addtid_b32 v162 offset:640\n"
    "v_mfma_f32_16x16x32_bf16 v[164:167], v[60:63], a[60:63], v[164:167]\n"
    "ds_write_addtid_b32 v163 offset:928\n"
    "s_waitcnt vmcnt(9)\n"
    "v_mfma_f32_16x16x32_bf16 v[168:171], v[64:67], a[0:3], 0\n"
    "v_mfma_f32_16x16x32_bf16 v[172:175], v[64:67], a[32:35], 0\n"
    "global_load_dwordx4 v[128:131], v248, s[14:15] offset:-2048 sc1\n"
    "s_waitcnt vmcnt(9)\n"
    "v_mfma_f32_16x16x32_bf16 v[168:171], v[68:71], a[4:7], v[168:171]\n"
    "ds_write_addtid_b32 v164 offset:1184\n"
    "v_mfma_f32_16x16x32_bf16 v[172:175], v[68:71], a[36:39], v[172:175]\n"
    "ds_write_addtid_b32 v165 offset:1472\n"
    "s_waitcnt vmcnt(8)\n"
    "v_mfma_f32_16x16x32_bf16 v[168:171], v[72:75], a[8:11], v[168:171]\n"
    "ds_write_addtid_b32 v166 offset:1824\n"
    "v_mfma_f32_16x16x32_bf16 v[172:175], v[72:75], a[40:43], v[172:175]\n"
    "ds_write_addtid_b32 v167 offset:2112\n"
    "s_waitcnt vmcnt(7)\n"
    "v_mfma_f32_16x16x32_bf16 v[168:171], v[76:79], a[12:15], v[168:171]\n"
    "global_load_dwordx4 v[132:135], v248, s[14:15] offset:-1024 sc1\n"
    "v_mfma_f32_16x16x32_bf16 v[172:175], v[76:79], a[44:47], v[172:175]\n"
    "s_waitcnt vmcnt(7)\n"
    "v_mfma_f32_16x16x32_bf16 v[168:171], v[80:83], a[16:19], v[168:171]\n"
    "s_waitcnt lgkmcnt(0)\n"
    "v_mfma_f32_16x16x32_bf16 v[172:175], v[80:83], a[48:51], v[172:175]\n"
    "s_waitcnt vmcnt(6)\n"
    "s_barrier\n"
    "v_mfma_f32_16x16x32_bf16 v[168:171], v[84:87], a[20:23], v[168:171]\n"
    "ds_read_b128 v[160:163], v13 offset:0\n"
    "global_load_dwordx4 v[104:107], v248, s[14:15] offset:2048 sc1\n"
    "v_mfma_f32_16x16x32_bf16 v[172:175], v[84:87], a[52:55], v[172:175]\n"
    "ds_read_b128 v[164:167], v13 offset:1184\n"
    "s_waitcnt vmcnt(6)\n"
    "v_mfma_f32_16x16x32_bf16 v[168:171], v[88:91], a[24:27], v[168:171]\n"
    "v_mfma_f32_16x16x32_bf16 v[172:175], v[88:91], a[56:59], v[172:175]\n"
    "global_load_dwordx4 v[108:111], v248, s[14:15] offset:3072 sc1\n"
    "s_waitcnt vmcnt(6)\n"
    "v_mfma_f32_16x16x32_bf16 v[168:171], v[92:95], a[28:31], v[168:171]\n"
    "v_mfma_f32_16x16x32_bf16 v[172:175], v[92:95], a[60:63], v[172:175]\n"
    "s_waitcnt lgkmcnt(1)\n"
    "v_mfma_f32_16x16x32_bf16 v[160:163], a[64:67], v[32:35], v[160:163]\n"
    "v_mfma_f32_16x16x32_bf16 v[160:163], a[68:71], v[36:39], v[160:163]\n"
    "ds_write_addtid_b32 v168 offset:2368\n"
    "global_load_dwordx4 v[136:139], v248, s[14:15] offset:0 sc1\n"
    "v_mfma_f32_16x16x32_bf16 v[160:163], a[72:75], v[40:43], v[160:163]\n"
    "ds_write_addtid_b32 v169 offset:2656\n"
    "v_mfma_f32_16x16x32_bf16 v[160:163], a[76:79], v[44:47], v[160:163]\n"
    "ds_write_addtid_b32 v170 offset:3008\n"
    "ds_write_addtid_b32 v172 offset:3552\n"
    "v_mfma_f32_16x16x32_bf16 v[160:163], a[80:83], v[48:51], v[160:163]\n"
    "ds_write_addtid_b32 v171 offset:3296\n"
    "v_mfma_f32_16x16x32_bf16 v[160:163], a[84:87], v[52:55], v[160:163]\n"
    "ds_write_addtid_b32 v173 offset:3840\n"
    "global_load_dwordx4 v[140:143], v248, s[14:15] offset:1024 sc1\n"
    "v_mfma_f32_16x16x32_bf16 v[160:163], a[88:91], v[56:59], v[160:163]\n"
    "ds_write_addtid_b32 v174 offset:4192\n"
    "ds_write_addtid_b32 v175 offset:4480\n"
    "v_mfma_f32_16x16x32_bf16 v[160:163], a[92:95], v[60:63], v[160:163]\n"
    "s_waitcnt lgkmcnt(8)\n"
    "v_mfma_f32_16x16x32_bf16 v[164:167], a[96:99], v[32:35], v[164:167]\n"
    "s_waitcnt lgkmcnt(0)\n"
    "v_mfma_f32_16x16x32_bf16 v[164:167], a[100:103], v[36:39], v[164:167]\n"
    "s_barrier\n"
    "v_mfma_f32_16x16x32_bf16 v[164:167], a[104:107], v[40:43], v[164:167]\n"
    "ds_read_b128 v[168:171], v13 offset:2368\n"
    "global_load_dwordx4 v[112:115], v250, s[14:15] offset:-4096 sc1\n"
    "v_mfma_f32_16x16x32_bf16 v[164:167], a[108:111], v[44:47], v[164:167]\n"
    "ds_read_b128 v[172:175], v13 offset:3552\n"
    "v_mfma_f32_16x16x32_bf16 v[164:167], a[112:115], v[48:51], v[164:167]\n"
    "ds_write_addtid_b32 v160 offset:0\n"
    "v_mfma_f32_16x16x32_bf16 v[164:167], a[116:119], v[52:55], v[164:167]\n"
    "ds_write_addtid_b32 v161 offset:288\n"
    "global_load_dwordx4 v[116:119], v250, s[14:15] offset:-3072 sc1\n"
    "v_mfma_f32_16x16x32_bf16 v[164:167], a[120:123], v[56:59], v[164:167]\n"
    "ds_write_addtid_b32 v162 offset:640\n"
    "v_mfma_f32_16x16x32_bf16 v[164:167], a[124:127], v[60:63], v[164:167]\n"
    "ds_write_addtid_b32 v163 offset:928\n"
    "s_waitcnt lgkmcnt(5)\n"
    "v_mfma_f32_16x16x32_bf16 v[168:171], a[64:67], v[64:67], v[168:171]\n"
    "v_mfma_f32_16x16x32_bf16 v[168:171], a[68:71], v[68:71], v[168:171]\n"
    "global_load_dwordx4 v[144:147], v250, s[14:15] offset:-2048 sc1\n"
    "v_mfma_f32_16x16x32_bf16 v[168:171], a[72:75], v[72:75], v[168:171]\n"
    "ds_write_addtid_b32 v164 offset:1184\n"
    "v_mfma_f32_16x16x32_bf16 v[168:171], a[76:79], v[76:79], v[168:171]\n"
    "ds_write_addtid_b32 v165 offset:1472\n"
    "v_mfma_f32_16x16x32_bf16 v[168:171], a[80:83], v[80:83], v[168:171]\n"
    "ds_write_addtid_b32 v166 offset:1824\n"
    "v_mfma_f32_16x16x32_bf16 v[168:171], a[84:87], v[84:87], v[168:171]\n"
    "ds_write_addtid_b32 v167 offset:2112\n"
    "global_load_dwordx4 v[148:151], v250, s[14:15] offset:-1024 sc1\n"
    "v_mfma_f32_16x16x32_bf16 v[168:171], a[88:91], v[88:91], v[168:171]\n"
    "v_mfma_f32_16x16x32_bf16 v[168:171], a[92:95], v[92:95], v[168:171]\n"
    "s_waitcnt lgkmcnt(8)\n"
    "v_mfma_f32_16x16x32_bf16 v[172:175], a[96:99], v[64:67], v[172:175]\n"
    "s_waitcnt lgkmcnt(0)\n"
    "v_mfma_f32_16x16x32_bf16 v[172:175], a[100:103], v[68:71], v[172:175]\n"
    "s_barrier\n"
    "v_mfma_f32_16x16x32_bf16 v[172:175], a[104:107], v[72:75], v[172:175]\n"
    "ds_read_b128 v[160:163], v13 offset:0\n"
    "global_load_dwordx4 v[120:123], v250, s[14:15] offset:2048 sc1\n"
    "v_mfma_f32_16x16x32_bf16 v[172:175], a[108:111], v[76:79], v[172:175]\n"
    "ds_read_b128 v[164:167], v13 offset:1184\n"
    "v_mfma_f32_16x16x32_bf16 v[172:175], a[112:115], v[80:83], v[172:175]\n"
    "ds_write_addtid_b32 v168 offset:2368\n"
    "v_mfma_f32_16x16x32_bf16 v[172:175], a[116:119], v[84:87], v[172:175]\n"
    "ds_write_addtid_b32 v169 offset:2656\n"
    "global_load_dwordx4 v[124:127], v250, s[14:15] offset:3072 sc1\n"
    "v_mfma_f32_16x16x32_bf16 v[172:175], a[120:123], v[88:91], v[172:175]\n"
    "ds_write_addtid_b32 v170 offset:3008\n"
    "v_mfma_f32_16x16x32_bf16 v[172:175], a[124:127], v[92:95], v[172:175]\n"
    "ds_write_addtid_b32 v171 offset:3296\n"
    "s_waitcnt lgkmcnt(5)\n"
    "v_mfma_f32_16x16x32_bf16 v[160:163], v[32:35], a[128:131], v[160:163]\n"
    "v_mfma_f32_16x16x32_bf16 v[160:163], v[36:39], a[132:135], v[160:163]\n"
    "global_load_dwordx4 v[152:155], v250, s[14:15] offset:0 sc1\n"
    "v_mfma_f32_16x16x32_bf16 v[160:163], v[40:43], a[136:139], v[160:163]\n"
    "ds_write_addtid_b32 v172 offset:3552\n"
    "v_mfma_f32_16x16x32_bf16 v[160:163], v[44:47], a[140:143], v[160:163]\n"
    "ds_write_addtid_b32 v173 offset:3840\n"
    "v_mfma_f32_16x16x32_bf16 v[160:163], v[48:51], a[144:147], v[160:163]\n"
    "ds_write_addtid_b32 v174 offset:4192\n"
    "v_mfma_f32_16x16x32_bf16 v[160:163], v[52:55], a[148:151], v[160:163]\n"
    "ds_write_addtid_b32 v175 offset:4480\n"
    "global_load_dwordx4 v[156:159], v250, s[14:15] offset:1024 sc1\n"
    "v_mfma_f32_16x16x32_bf16 v[160:163], v[56:59], a[152:155], v[160:163]\n"
    "v_mfma_f32_16x16x32_bf16 v[160:163], v[60:63], a[156:159], v[160:163]\n"
    "s_waitcnt lgkmcnt(8)\n"
    "v_mfma_f32_16x16x32_bf16 v[164:167], v[32:35], a[160:163], v[164:167]\n"
    "global_load_dwordx2 v[252:253], v9, s[8:9]\n"
    "s_waitcnt lgkmcnt(0)\n"
    "v_mfma_f32_16x16x32_bf16 v[164:167], v[36:39], a[164:167], v[164:167]\n"
    "s_barrier\n"
    "v_mfma_f32_16x16x32_bf16 v[164:167], v[40:43], a[168:171], v[164:167]\n"
    "ds_read_b128 v[168:171], v13 offset:2368\n"
    "v_mfma_f32_16x16x32_bf16 v[164:167], v[44:47], a[172:175], v[164:167]\n"
    "ds_read_b128 v[172:175], v13 offset:3552\n"
    "v_or_b32 v22, 2, v21\n"
    "v_mfma_f32_16x16x32_bf16 v[164:167], v[48:51], a[176:179], v[164:167]\n"
    "ds_write_addtid_b32 v160 offset:0\n"
    "global_load_dwordx2 v[254:255], v9, s[10:11]\n"
    "v_mfma_f32_16x16x32_bf16 v[164:167], v[52:55], a[180:183], v[164:167]\n"
    "ds_write_addtid_b32 v161 offset:288\n"
    "v_mfma_f32_16x16x32_bf16 v[164:167], v[56:59], a[184:187], v[164:167]\n"
    "ds_write_addtid_b32 v162 offset:640\n"
    "v_mfma_f32_16x16x32_bf16 v[164:167], v[60:63], a[188:191], v[164:167]\n"
    "ds_write_addtid_b32 v163 offset:928\n"
    "s_waitcnt lgkmcnt(5)\n"
    "v_mfma_f32_16x16x32_bf16 v[168:171], v[64:67], a[128:131], v[168:171]\n"
    "s_mov_b64 exec, s[66:67]\n"
    "global_store_dword v18, v22, s[14:15] sc1\n"
    "s_mov_b64 exec, -1\n"
    "v_mfma_f32_16x16x32_bf16 v[168:171], v[68:71], a[132:135], v[168:171]\n"
    "global_load_dwordx4 v[192:195], v8, s[36:37] nt\n"
    "v_mfma_f32_16x16x32_bf16 v[168:171], v[72:75], a[136:139], v[168:171]\n"
    "ds_write_addtid_b32 v164 offset:1184\n"
    "v_mfma_f32_16x16x32_bf16 v[168:171], v[76:79], a[140:143], v[168:171]\n"
    "ds_write_addtid_b32 v165 offset:1472\n"
    "v_mfma_f32_16x16x32_bf16 v[168:171], v[80:83], a[144:147], v[168:171]\n"
    "ds_write_addtid_b32 v166 offset:1824\n"
    "v_mfma_f32_16x16x32_bf16 v[168:171], v[84:87], a[148:151], v[168:171]\n"
    "ds_write_addtid_b32 v167 offset:2112\n"
    "global_load_dwordx4 v[196:199], v8, s[38:39] nt\n"
    "v_mfma_f32_16x16x32_bf16 v[168:171], v[88:91], a[152:155], v[168:171]\n"
    "v_mfma_f32_16x16x32_bf16 v[168:171], v[92:95], a[156:159], v[168:171]\n"
    "s_waitcnt lgkmcnt(8)\n"
    "v_mfma_f32_16x16x32_bf16 v[172:175], v[64:67], a[160:163], v[172:175]\n"
    "s_waitcnt lgkmcnt(0)\n"
    "v_mfma_f32_16x16x32_bf16 v[172:175], v[68:71], a[164:167], v[172:175]\n"
    "s_barrier\n"
    "v_mfma_f32_16x16x32_bf16 v[172:175], v[72:75], a[168:171], v[172:175]\n"
    "ds_read_b128 v[160:163], v13 offset:0\n"
    "global_load_dwordx4 v[200:203], v8, s[40:41] nt\n"
    "v_mfma_f32_16x16x32_bf16 v[172:175], v[76:79], a[172:175], v[172:175]\n"
    "ds_read_b128 v[164:167], v13 offset:1184\n"
    "v_mfma_f32_16x16x32_bf16 v[172:175], v[80:83], a[176:179], v[172:175]\n"
    "ds_write_addtid_b32 v168 offset:2368\n"
    "v_mfma_f32_16x16x32_bf16 v[172:175], v[84:87], a[180:183], v[172:175]\n"
    "ds_write_addtid_b32 v169 offset:2656\n"
    "v_mfma_f32_16x16x32_bf16 v[172:175], v[88:91], a[184:187], v[172:175]\n"
    "ds_write_addtid_b32 v170 offset:3008\n"
    "global_load_dwordx4 v[204:207], v8, s[42:43] nt\n"
    "v_mfma_f32_16x16x32_bf16 v[172:175], v[92:95], a[188:191], v[172:175]\n"
    "ds_write_addtid_b32 v171 offset:3296\n"
    "s_waitcnt lgkmcnt(5)\n"
    "v_mfma_f32_16x16x32_bf16 v[160:163], a[192:195], v[32:35], v[160:163]\n"
    "v_mfma_f32_16x16x32_bf16 v[160:163], a[196:199], v[36:39], v[160:163]\n"
    "v_mfma_f32_16x16x32_bf16 v[160:163], a[200:203], v[40:43], v[160:163]\n"
    "ds_write_addtid_b32 v172 offset:3552\n"
    "global_load_dwordx4 v[208:211], v8, s[44:45] nt\n"
    "v_mfma_f32_16x16x32_bf16 v[160:163], a[204:207], v[44:47], v[160:163]\n"
    "ds_write_addtid_b32 v173 offset:3840\n"
    "v_mfma_f32_16x16x32_bf16 v[160:163], a[208:211], v[48:51], v[160:163]\n"
    "ds_write_addtid_b32 v174 offset:4192\n"
    "v_mfma_f32_16x16x32_bf16 v[160:163], a[212:215], v[52:55], v[160:163]\n"
    "ds_write_addtid_b32 v175 offset:4480\n"
    "v_mfma_f32_16x16x32_bf16 v[160:163], a[216:219], v[56:59], v[160:163]\n"
    "global_load_dwordx4 v[212:215], v8, s[46:47] nt\n"
    "v_mfma_f32_16x16x32_bf16 v[160:163], a[220:223], v[60:63], v[160:163]\n"
    "s_waitcnt lgkmcnt(8)\n"
    "v_mfma_f32_16x16x32_bf16 v[164:167], a[224:227], v[32:35], v[164:167]\n"
    "s_waitcnt lgkmcnt(0)\n"
    "v_mfma_f32_16x16x32_bf16 v[164:167], a[228:231], v[36:39], v[164:167]\n"
    "s_barrier\n"
    "v_mfma_f32_16x16x32_bf16 v[164:167], a[232:235], v[40:43], v[164:167]\n"
    "ds_read_b128 v[168:171], v13 offset:2368\n"
    "global_load_dwordx4 v[216:219], v8, s[48:49] nt\n"
    "v_mfma_f32_16x16x32_bf16 v[164:167], a[236:239], v[44:47], v[164:167]\n"
    "ds_read_b128 v[172:175], v13 offset:3552\n"
    "v_mfma_f32_16x16x32_bf16 v[164:167], a[240:243], v[48:51], v[164:167]\n"
    "v_mfma_f32_16x16x32_bf16 v[164:167], a[244:247], v[52:55], v[164:167]\n"
    "v_mfma_f32_16x16x32_bf16 v[164:167], a[248:251], v[56:59], v[164:167]\n"
    "global_load_dwordx4 v[220:223], v8, s[50:51] nt\n"
    "v_mfma_f32_16x16x32_bf16 v[164:167], a[252:255], v[60:63], v[164:167]\n"
    "s_waitcnt lgkmcnt(1)\n"
    "v_mfma_f32_16x16x32_bf16 v[168:171], a[192:195], v[64:67], v[168:171]\n"
    "v_mfma_f32_16x16x32_bf16 v[168:171], a[196:199], v[68:71], v[168:171]\n"
    "v_mfma_f32_16x16x32_bf16 v[168:171], a[200:203], v[72:75], v[168:171]\n"
    "v_mfma_f32_16x16x32_bf16 v[168:171], a[204:207], v[76:79], v[168:171]\n"
    "v_mfma_f32_16x16x32_bf16 v[168:171], a[208:211], v[80:83], v[168:171]\n"
    "v_mfma_f32_16x16x32_bf16 v[168:171], a[212:215], v[84:87], v[168:171]\n"
    "v_mfma_f32_16x16x32_bf16 v[168:171], a[216:219], v[88:91], v[168:171]\n"
    "v_mfma_f32_16x16x32_bf16 v[168:171], a[220:223], v[92:95], v[168:171]\n"
    "s_waitcnt lgkmcnt(0)\n"
    "v_mfma_f32_16x16x32_bf16 v[172:175], a[224:227], v[64:67], v[172:175]\n"
    "v_mfma_f32_16x16x32_bf16 v[172:175], a[228:231], v[68:71], v[172:175]\n"
    "v_mfma_f32_16x16x32_bf16 v[172:175], a[232:235], v[72:75], v[172:175]\n"
    "v_cvt_pk_bf16_f32 v176, v160, v168\n"
    "v_cvt_pk_bf16_f32 v178, v161, v169\n"
    "v_mfma_f32_16x16x32_bf16 v[172:175], a[236:239], v[76:79], v[172:175]\n"
    "v_cvt_pk_bf16_f32 v180, v162, v170\n"
    "v_cvt_pk_bf16_f32 v182, v163, v171\n"
    "v_mfma_f32_16x16x32_bf16 v[172:175], a[240:243], v[80:83], v[172:175]\n"
    "v_mfma_f32_16x16x32_bf16 v[172:175], a[244:247], v[84:87], v[172:175]\n"
    "v_mfma_f32_16x16x32_bf16 v[172:175], a[248:251], v[88:91], v[172:175]\n"
    "v_mfma_f32_16x16x32_bf16 v[172:175], a[252:255], v[92:95], v[172:175]\n"
    "s_waitcnt vmcnt(12)\n"
    "v_mfma_f32_16x16x32_bf16 v[160:163], v[96:99], a[0:3], 0\n"
    "v_mfma_f32_16x16x32_bf16 v[160:163], v[100:103], a[4:7], v[160:163]\n"
    "v_mfma_f32_16x16x32_bf16 v[160:163], v[104:107], a[8:11], v[160:163]\n"
    "v_cvt_pk_bf16_f32 v184, v164, v172\n"
    "v_cvt_pk_bf16_f32 v186, v165, v173\n"
    "v_mfma_f32_16x16x32_bf16 v[160:163], v[108:111], a[12:15], v[160:163]\n"
    "v_cvt_pk_bf16_f32 v188, v166, v174\n"
    "v_cvt_pk_bf16_f32 v190, v167, v175\n"
    "v_mfma_f32_16x16x32_bf16 v[160:163], v[112:115], a[16:19], v[160:163]\n"
    "v_mfma_f32_16x16x32_bf16 v[160:163], v[116:119], a[20:23], v[160:163]\n"
    "v_mfma_f32_16x16x32_bf16 v[160:163], v[120:123], a[24:27], v[160:163]\n"
    "v_mfma_f32_16x16x32_bf16 v[160:163], v[124:127], a[28:31], v[160:163]\n"
    "s_waitcnt vmcnt(10)\n"
    "v_mfma_f32_16x16x32_bf16 v[164:167], v[128:131], a[0:3], 0\n"
    "v_mfma_f32_16x16x32_bf16 v[164:167], v[132:135], a[4:7], v[164:167]\n"
    "v_mfma_f32_16x16x32_bf16 v[164:167], v[136:139], a[8:11], v[164:167]\n"
    "v_mfma_f32_16x16x32_bf16 v[164:167], v[140:143], a[12:15], v[164:167]\n"
    "v_mfma_f32_16x16x32_bf16 v[164:167], v[144:147], a[16:19], v[164:167]\n"
    "ds_write_addtid_b32 v160 offset:0\n"
    "v_mfma_f32_16x16x32_bf16 v[164:167], v[148:151], a[20:23], v[164:167]\n"
    "ds_write_addtid_b32 v161 offset:288\n"
    "v_mfma_f32_16x16x32_bf16 v[164:167], v[152:155], a[24:27], v[164:167]\n"
    "ds_write_addtid_b32 v162 offset:640\n"
    "v_mfma_f32_16x16x32_bf16 v[164:167], v[156:159], a[28:31], v[164:167]\n"
    "ds_write_addtid_b32 v163 offset:928\n"
    "v_mfma_f32_16x16x32_bf16 v[168:171], v[96:99], a[32:35], 0\n"
    "v_mfma_f32_16x16x32_bf16 v[168:171], v[100:103], a[36:39], v[168:171]\n"
    "v_mfma_f32_16x16x32_bf16 v[168:171], v[104:107], a[40:43], v[168:171]\n"
    "ds_write_addtid_b32 v164 offset:1184\n"
    "v_mfma_f32_16x16x32_bf16 v[168:171], v[108:111], a[44:47], v[168:171]\n"
    "ds_write_addtid_b32 v165 offset:1472\n"
    "v_mfma_f32_16x16x32_bf16 v[168:171], v[112:115], a[48:51], v[168:171]\n"
    "ds_write_addtid_b32 v166 offset:1824\n"
    "v_mfma_f32_16x16x32_bf16 v[168:171], v[116:119], a[52:55], v[168:171]\n"
    "ds_write_addtid_b32 v167 offset:2112\n"
    "v_mfma_f32_16x16x32_bf16 v[168:171], v[120:123], a[56:59], v[168:171]\n"
    "v_mfma_f32_16x16x32_bf16 v[168:171], v[124:127], a[60:63], v[168:171]\n"
    "v_mfma_f32_16x16x32_bf16 v[172:175], v[128:131], a[32:35], 0\n"
    "s_waitcnt lgkmcnt(0)\n"
    "v_mfma_f32_16x16x32_bf16 v[172:175], v[132:135], a[36:39], v[172:175]\n"
    "s_barrier\n"
    "v_mfma_f32_16x16x32_bf16 v[172:175], v[136:139], a[40:43], v[172:175]\n"
    "ds_read_b128 v[160:163], v13 offset:0\n"
    "v_mfma_f32_16x16x32_bf16 v[172:175], v[140:143], a[44:47], v[172:175]\n"
    "ds_read_b128 v[164:167], v13 offset:1184\n"
    "v_mfma_f32_16x16x32_bf16 v[172:175], v[144:147], a[48:51], v[172:175]\n"
    "ds_write_addtid_b32 v168 offset:2368\n"
    "v_mfma_f32_16x16x32_bf16 v[172:175], v[148:151], a[52:55], v[172:175]\n"
    "ds_write_addtid_b32 v169 offset:2656\n"
    "v_add_u32 v248, 131072, v248\n"
    "v_mfma_f32_16x16x32_bf16 v[172:175], v[152:155], a[56:59], v[172:175]\n"
    "ds_write_addtid_b32 v170 offset:3008\n"
    "v_mfma_f32_16x16x32_bf16 v[172:175], v[156:159], a[60:63], v[172:175]\n"
    "ds_write_addtid_b32 v171 offset:3296\n"
    "s_waitcnt lgkmcnt(5)\n"
    "v_mfma_f32_16x16x32_bf16 v[160:163], a[64:67], v[96:99], v[160:163]\n"
    "s_waitcnt vmcnt(8)\n"
    "v_lshlrev_b32 v240, 16, v252\n"
    "v_mfma_f32_16x16x32_bf16 v[160:163], a[68:71], v[100:103], v[160:163]\n"
    "global_load_dwordx4 v[32:35], v248, s[14:15] offset:-4096 sc1\n"
    "v_add_u32 v250, 8192, v248\n"
    "v_mfma_f32_16x16x32_bf16 v[160:163], a[72:75], v[104:107], v[160:163]\n"
    "ds_write_addtid_b32 v172 offset:3552\n"
    "v_and_b32 v241, 0xffff0000, v252\n"
    "v_mfma_f32_16x16x32_bf16 v[160:163], a[76:79], v[108:111], v[160:163]\n"
    "ds_write_addtid_b32 v173 offset:3840\n"
    "v_lshlrev_b32 v242, 16, v253\n"
    "v_mfma_f32_16x16x32_bf16 v[160:163], a[80:83], v[112:115], v[160:163]\n"
    "ds_write_addtid_b32 v174 offset:4192\n"
    "v_and_b32 v243, 0xffff0000, v253\n"
    "v_mfma_f32_16x16x32_bf16 v[160:163], a[84:87], v[116:119], v[160:163]\n"
    "ds_write_addtid_b32 v175 offset:4480\n"
    "global_load_dwordx4 v[36:39], v248, s[14:15] offset:-3072 sc1\n"
    "v_mfma_f32_16x16x32_bf16 v[160:163], a[88:91], v[120:123], v[160:163]\n"
    "v_lshlrev_b32 v244, 16, v254\n"
    "v_and_b32 v245, 0xffff0000, v254\n"
    "v_mfma_f32_16x16x32_bf16 v[160:163], a[92:95], v[124:127], v[160:163]\n"
    "v_lshlrev_b32 v246, 16, v255\n"
    "s_waitcnt lgkmcnt(8)\n"
    "v_mfma_f32_16x16x32_bf16 v[164:167], a[64:67], v[128:131], v[164:167]\n"
    "v_and_b32 v247, 0xffff0000, v255\n"
    "s_waitcnt lgkmcnt(0)\n"
    "v_mfma_f32_16x16x32_bf16 v[164:167], a[68:71], v[132:135], v[164:167]\n"
    "s_barrier\n"
    "v_mfma_f32_16x16x32_bf16 v[164:167], a[72:75], v[136:139], v[164:167]\n"
    "ds_read_b128 v[168:171], v13 offset:2368\n"
    "global_load_dwordx4 v[64:67], v248, s[14:15] offset:-2048 sc1\n"
    "v_mfma_f32_16x16x32_bf16 v[164:167], a[76:79], v[140:143], v[164:167]\n"
    "ds_read_b128 v[172:175], v13 offset:3552\n"
    "v_mul_f32_e64 v244, -v244, v240\n"
    "v_mfma_f32_16x16x32_bf16 v[164:167], a[80:83], v[144:147], v[164:167]\n"
    "ds_write_addtid_b32 v160 offset:0\n"
    "v_mul_f32_e64 v245, -v245, v241\n"
    "v_mfma_f32_16x16x32_bf16 v[164:167], a[84:87], v[148:151], v[164:167]\n"
    "ds_write_addtid_b32 v161 offset:288\n"
    "global_load_dwordx4 v[68:71], v248, s[14:15] offset:-1024 sc1\n"
    "v_mfma_f32_16x16x32_bf16 v[164:167], a[88:91], v[152:155], v[164:167]\n"
    "ds_write_addtid_b32 v162 offset:640\n"
    "v_mul_f32_e64 v246, -v246, v242\n"
    "v_mfma_f32_16x16x32_bf16 v[164:167], a[92:95], v[156:159], v[164:167]\n"
    "ds_write_addtid_b32 v163 offset:928\n"
    "s_waitcnt lgkmcnt(5)\n"
    "v_mfma_f32_16x16x32_bf16 v[168:171], a[96:99], v[96:99], v[168:171]\n"
    "v_mul_f32_e64 v247, -v247, v243\n"
    "s_waitcnt vmcnt(10)\n"
    "v_mfma_f32_16x16x32_bf16 v[168:171], a[100:103], v[100:103], v[168:171]\n"
    "global_load_dwordx4 v[40:43], v248, s[14:15] offset:2048 sc1\n"
    "v_cvt_f32_i32 v192, v192\n"
    "v_mfma_f32_16x16x32_bf16 v[168:171], a[104:107], v[104:107], v[168:171]\n"
    "ds_write_addtid_b32 v164 offset:1184\n"
    "v_cvt_f32_i32 v193, v193\n"
    "v_mfma_f32_16x16x32_bf16 v[168:171], a[108:111], v[108:111], v[168:171]\n"
    "ds_write_addtid_b32 v165 offset:1472\n"
    "v_cvt_f32_i32 v194, v194\n"
    "v_mfma_f32_16x16x32_bf16 v[168:171], a[112:115], v[112:115], v[168:171]\n"
    "ds_write_addtid_b32 v166 offset:1824\n"
    "v_cvt_f32_i32 v195, v195\n"
    "v_mfma_f32_16x16x32_bf16 v[168:171], a[116:119], v[116:119], v[168:171]\n"
    "ds_write_addtid_b32 v167 offset:2112\n"
    "global_load_dwordx4 v[44:47], v248, s[14:15] offset:3072 sc1\n"
    "v_mfma_f32_16x16x32_bf16 v[168:171], a[120:123], v[120:123], v[168:171]\n"
    "v_cvt_f32_i32 v196, v196\n"
    "v_cvt_f32_i32 v197, v197\n"
    "v_mfma_f32_16x16x32_bf16 v[168:171], a[124:127], v[124:127], v[168:171]\n"
    "v_cvt_f32_i32 v198, v198\n"
    "s_waitcnt lgkmcnt(8)\n"
    "v_mfma_f32_16x16x32_bf16 v[172:175], a[96:99], v[128:131], v[172:175]\n"
    "v_cvt_f32_i32 v199, v199\n"
    "s_waitcnt lgkmcnt(0)\n"
    "v_mfma_f32_16x16x32_bf16 v[172:175], a[100:103], v[132:135], v[172:175]\n"
    "s_barrier\n"
    "v_mfma_f32_16x16x32_bf16 v[172:175], a[104:107], v[136:139], v[172:175]\n"
    "ds_read_b128 v[160:163], v13 offset:0\n"
    "global_load_dwordx4 v[72:75], v248, s[14:15] offset:0 sc1\n"
    "v_mfma_f32_16x16x32_bf16 v[172:175], a[108:111], v[140:143], v[172:175]\n"
    "ds_read_b128 v[164:167], v13 offset:1184\n"
    "v_pk_fma_f32 v[192:193], v[192:193], v[240:241], v[244:245]\n"
    "v_mfma_f32_16x16x32_bf16 v[172:175], a[112:115], v[144:147], v[172:175]\n"
    "ds_write_addtid_b32 v168 offset:2368\n"
    "v_pk_fma_f32 v[194:195], v[194:195], v[242:243], v[246:247]\n"
    "v_mfma_f32_16x16x32_bf16 v[172:175], a[116:119], v[148:151], v[172:175]\n"
    "ds_write_addtid_b32 v169 offset:2656\n"
    "global_load_dwordx4 v[76:79], v248, s[14:15] offset:1024 sc1\n"
    "v_mfma_f32_16x16x32_bf16 v[172:175], a[120:123], v[152:155], v[172:175]\n"
    "ds_write_addtid_b32 v170 offset:3008\n"
    "v_pk_fma_f32 v[196:197], v[196:197], v[240:241], v[244:245]\n"
    "v_mfma_f32_16x16x32_bf16 v[172:175], a[124:127], v[156:159], v[172:175]\n"
    "ds_write_addtid_b32 v171 offset:3296\n"
    "s_waitcnt lgkmcnt(5)\n"
    "v_mfma_f32_16x16x32_bf16 v[160:163], v[96:99], a[128:131], v[160:163]\n"
    "v_pk_fma_f32 v[198:199], v[198:199], v[242:243], v[246:247]\n"
    "v_cvt_pk_bf16_f32 v224, v192, v196\n"
    "v_mfma_f32_16x16x32_bf16 v[160:163], v[100:103], a[132:135], v[160:163]\n"
    "global_load_dwordx4 v[48:51], v250, s[14:15] offset:-4096 sc1\n"
    "v_cvt_pk_bf16_f32 v228, v193, v197\n"
    "v_mfma_f32_16x16x32_bf16 v[160:163], v[104:107], a[136:139], v[160:163]\n"
    "ds_write_addtid_b32 v172 offset:3552\n"
    "v_cvt_pk_bf16_f32 v232, v194, v198\n"
    "v_mfma_f32_16x16x32_bf16 v[160:163], v[108:111], a[140:143], v[160:163]\n"
    "ds_write_addtid_b32 v173 offset:3840\n"
    "v_cvt_pk_bf16_f32 v236, v195, v199\n"
    "v_mfma_f32_16x16x32_bf16 v[160:163], v[112:115], a[144:147], v[160:163]\n"
    "ds_write_addtid_b32 v174 offset:4192\n"
    "s_waitcnt vmcnt(13)\n"
    "v_mfma_f32_16x16x32_bf16 v[160:163], v[116:119], a[148:151], v[160:163]\n"
    "ds_write_addtid_b32 v175 offset:4480\n"
    "global_load_dwordx4 v[52:55], v250, s[14:15] offset:-3072 sc1\n"
    "v_mfma_f32_16x16x32_bf16 v[160:163], v[120:123], a[152:155], v[160:163]\n"
    "v_cvt_f32_i32 v200, v200\n"
    "v_cvt_f32_i32 v201, v201\n"
    "v_mfma_f32_16x16x32_bf16 v[160:163], v[124:127], a[156:159], v[160:163]\n"
    "v_cvt_f32_i32 v202, v202\n"
    "s_waitcnt lgkmcnt(8)\n"
    "v_mfma_f32_16x16x32_bf16 v[164:167], v[128:131], a[128:131], v[164:167]\n"
    "v_cvt_f32_i32 v203, v203\n"
    "s_waitcnt lgkmcnt(0)\n"
    "v_mfma_f32_16x16x32_bf16 v[164:167], v[132:135], a[132:135], v[164:167]\n"
    "s_barrier\n"
    "v_mfma_f32_16x16x32_bf16 v[164:167], v[136:139], a[136:139], v[164:167]\n"
    "ds_read_b128 v[168:171], v13 offset:2368\n"
    "global_load_dwordx4 v[80:83], v250, s[14:15] offset:-2048 sc1\n"
    "v_mfma_f32_16x16x32_bf16 v[164:167], v[140:143], a[140:143], v[164:167]\n"
    "ds_read_b128 v[172:175], v13 offset:3552\n"
    "v_cvt_f32_i32 v204, v204\n"
    "v_mfma_f32_16x16x32_bf16 v[164:167], v[144:147], a[144:147], v[164:167]\n"
    "ds_write_addtid_b32 v160 offset:0\n"
    "v_cvt_f32_i32 v205, v205\n"
    "v_mfma_f32_16x16x32_bf16 v[164:167], v[148:151], a[148:151], v[164:167]\n"
    "ds_write_addtid_b32 v161 offset:288\n"
    "global_load_dwordx4 v[84:87], v250, s[14:15] offset:-1024 sc1\n"
    "v_mfma_f32_16x16x32_bf16 v[164:167], v[152:155], a[152:155], v[164:167]\n"
    "ds_write_addtid_b32 v162 offset:640\n"
    "v_cvt_f32_i32 v206, v206\n"
    "v_mfma_f32_16x16x32_bf16 v[164:167], v[156:159], a[156:159], v[164:167]\n"
    "ds_write_addtid_b32 v163 offset:928\n"
    "s_waitcnt lgkmcnt(5)\n"
    "v_mfma_f32_16x16x32_bf16 v[168:171], v[96:99], a[160:163], v[168:171]\n"
    "v_cvt_f32_i32 v207, v207\n"
    "v_pk_fma_f32 v[200:201], v[200:201], v[240:241], v[244:245]\n"
    "v_mfma_f32_16x16x32_bf16 v[168:171], v[100:103], a[164:167], v[168:171]\n"
    "global_load_dwordx4 v[56:59], v250, s[14:15] offset:2048 sc1\n"
    "v_pk_fma_f32 v[202:203], v[202:203], v[242:243], v[246:247]\n"
    "v_mfma_f32_16x16x32_bf16 v[168:171], v[104:107], a[168:171], v[168:171]\n"
    "ds_write_addtid_b32 v164 offset:1184\n"
    "v_pk_fma_f32 v[204:205], v[204:205], v[240:241], v[244:245]\n"
    "v_mfma_f32_16x16x32_bf16 v[168:171], v[108:111], a[172:175], v[168:171]\n"
    "ds_write_addtid_b32 v165 offset:1472\n"
    "v_pk_fma_f32 v[206:207], v[206:207], v[242:243], v[246:247]\n"
    "v_mfma_f32_16x16x32_bf16 v[168:171], v[112:115], a[176:179], v[168:171]\n"
    "ds_write_addtid_b32 v166 offset:1824\n"
    "v_cvt_pk_bf16_f32 v225, v200, v204\n"
    "v_mfma_f32_16x16x32_bf16 v[168:171], v[116:119], a[180:183], v[168:171]\n"
    "ds_write_addtid_b32 v167 offset:2112\n"
    "global_load_dwordx4 v[60:63], v250, s[14:15] offset:3072 sc1\n"
    "v_mfma_f32_16x16x32_bf16 v[168:171], v[120:123], a[184:187], v[168:171]\n"
    "v_cvt_pk_bf16_f32 v229, v201, v205\n"
    "v_cvt_pk_bf16_f32 v233, v202, v206\n"
    "v_mfma_f32_16x16x32_bf16 v[168:171], v[124:127], a[188:191], v[168:171]\n"
    "v_cvt_pk_bf16_f32 v237, v203, v207\n"
    "s_waitcnt lgkmcnt(8)\n"
    "v_mfma_f32_16x16x32_bf16 v[172:175], v[128:131], a[160:163], v[172:175]\n"
    "s_waitcnt vmcnt(16)\n"
    "s_waitcnt lgkmcnt(0)\n"
    "v_mfma_f32_16x16x32_bf16 v[172:175], v[132:135], a[164:167], v[172:175]\n"
    "s_barrier\n"
    "v_mfma_f32_16x16x32_bf16 v[172:175], v[136:139], a[168:171], v[172:175]\n"
    "ds_read_b128 v[160:163], v13 offset:0\n"
    "global_load_dwordx4 v[88:91], v250, s[14:15] offset:0 sc1\n"
    "v_mfma_f32_16x16x32_bf16 v[172:175], v[140:143], a[172:175], v[172:175]\n"
    "ds_read_b128 v[164:167], v13 offset:1184\n"
    "v_cvt_f32_i32 v208, v208\n"
    "v_mfma_f32_16x16x32_bf16 v[172:175], v[144:147], a[176:179], v[172:175]\n"
    "ds_write_addtid_b32 v168 offset:2368\n"
    "v_cvt_f32_i32 v209, v209\n"
    "v_mfma_f32_16x16x32_bf16 v[172:175], v[148:151], a[180:183], v[172:175]\n"
    "ds_write_addtid_b32 v169 offset:2656\n"
    "global_load_dwordx4 v[92:95], v250, s[14:15] offset:1024 sc1\n"
    "v_mfma_f32_16x16x32_bf16 v[172:175], v[152:155], a[184:187], v[172:175]\n"
    "ds_write_addtid_b32 v170 offset:3008\n"
    "v_cvt_f32_i32 v210, v210\n"
    "v_mfma_f32_16x16x32_bf16 v[172:175], v[156:159], a[188:191], v[172:175]\n"
    "ds_write_addtid_b32 v171 offset:3296\n"
    "s_waitcnt lgkmcnt(5)\n"
    "v_mfma_f32_16x16x32_bf16 v[160:163], a[192:195], v[96:99], v[160:163]\n"
    "v_cvt_f32_i32 v211, v211\n"
    "v_cvt_f32_i32 v212, v212\n"
    "v_mfma_f32_16x16x32_bf16 v[160:163], a[196:199], v[100:103], v[160:163]\n"
    "v_cvt_f32_i32 v213, v213\n"
    "v_cvt_f32_i32 v214, v214\n"
    "v_mfma_f32_16x16x32_bf16 v[160:163], a[200:203], v[104:107], v[160:163]\n"
    "ds_write_addtid_b32 v172 offset:3552\n"
    "v_cvt_f32_i32 v215, v215\n"
    "v_mfma_f32_16x16x32_bf16 v[160:163], a[204:207], v[108:111], v[160:163]\n"
    "ds_write_addtid_b32 v173 offset:3840\n"
    "v_pk_fma_f32 v[208:209], v[208:209], v[240:241], v[244:245]\n"
    "v_mfma_f32_16x16x32_bf16 v[160:163], a[208:211], v[112:115], v[160:163]\n"
    "ds_write_addtid_b32 v174 offset:4192\n"
    "v_pk_fma_f32 v[210:211], v[210:211], v[242:243], v[246:247]\n"
    "v_mfma_f32_16x16x32_bf16 v[160:163], a[212:215], v[116:119], v[160:163]\n"
    "ds_write_addtid_b32 v175 offset:4480\n"
    "v_pk_fma_f32 v[212:213], v[212:213], v[240:241], v[244:245]\n"
    "v_mfma_f32_16x16x32_bf16 v[160:163], a[216:219], v[120:123], v[160:163]\n"
    "v_pk_fma_f32 v[214:215], v[214:215], v[242:243], v[246:247]\n"
    "v_cvt_pk_bf16_f32 v226, v208, v212\n"
    "v_mfma_f32_16x16x32_bf16 v[160:163], a[220:223], v[124:127], v[160:163]\n"
    "v_cvt_pk_bf16_f32 v230, v209, v213\n"
    "s_waitcnt lgkmcnt(8)\n"
    "v_mfma_f32_16x16x32_bf16 v[164:167], a[192:195], v[128:131], v[164:167]\n"
    "v_cvt_pk_bf16_f32 v234, v210, v214\n"
    "s_waitcnt lgkmcnt(0)\n"
    "v_mfma_f32_16x16x32_bf16 v[164:167], a[196:199], v[132:135], v[164:167]\n"
    "s_barrier\n"
    "v_mfma_f32_16x16x32_bf16 v[164:167], a[200:203], v[136:139], v[164:167]\n"
    "ds_read_b128 v[168:171], v13 offset:2368\n"
    "v_cvt_pk_bf16_f32 v238, v211, v215\n"
    "v_mfma_f32_16x16x32_bf16 v[164:167], a[204:207], v[140:143], v[164:167]\n"
    "ds_read_b128 v[172:175], v13 offset:3552\n"
    "s_waitcnt vmcnt(16)\n"
    "v_mfma_f32_16x16x32_bf16 v[164:167], a[208:211], v[144:147], v[164:167]\n"
    "v_cvt_f32_i32 v216, v216\n"
    "v_cvt_f32_i32 v217, v217\n"
    "v_mfma_f32_16x16x32_bf16 v[164:167], a[212:215], v[148:151], v[164:167]\n"
    "v_cvt_f32_i32 v218, v218\n"
    "v_cvt_f32_i32 v219, v219\n"
    "v_mfma_f32_16x16x32_bf16 v[164:167], a[216:219], v[152:155], v[164:167]\n"
    "v_cvt_f32_i32 v220, v220\n"
    "v_cvt_f32_i32 v221, v221\n"
    "v_mfma_f32_16x16x32_bf16 v[164:167], a[220:223], v[156:159], v[164:167]\n"
    "v_cvt_f32_i32 v222, v222\n"
    "s_waitcnt lgkmcnt(1)\n"
    "v_mfma_f32_16x16x32_bf16 v[168:171], a[224:227], v[96:99], v[168:171]\n"
    "v_cvt_f32_i32 v223, v223\n"
    "v_pk_fma_f32 v[216:217], v[216:217], v[240:241], v[244:245]\n"
    "v_mfma_f32_16x16x32_bf16 v[168:171], a[228:231], v[100:103], v[168:171]\n"
    "v_pk_fma_f32 v[218:219], v[218:219], v[242:243], v[246:247]\n"
    "v_pk_fma_f32 v[220:221], v[220:221], v[240:241], v[244:245]\n"
    "v_mfma_f32_16x16x32_bf16 v[168:171], a[232:235], v[104:107], v[168:171]\n"
    "v_cvt_pk_bf16_f32 v177, v160, v164\n"
    "v_cvt_pk_bf16_f32 v179, v161, v165\n"
    "v_mfma_f32_16x16x32_bf16 v[168:171], a[236:239], v[108:111], v[168:171]\n"
    "v_cvt_pk_bf16_f32 v181, v162, v166\n"
    "v_cvt_pk_bf16_f32 v183, v163, v167\n"
    "v_mfma_f32_16x16x32_bf16 v[168:171], a[240:243], v[112:115], v[168:171]\n"
    "v_pk_fma_f32 v[222:223], v[222:223], v[242:243], v[246:247]\n"
    "v_cvt_pk_bf16_f32 v227, v216, v220\n"
    "HOP1_%=:\n"
    "s_cbranch_vccnz HOP2_%=\n"
    "v_mfma_f32_16x16x32_bf16 v[168:171], a[244:247], v[116:119], v[168:171]\n"
    "v_cvt_pk_bf16_f32 v231, v217, v221\n"
    "v_cvt_pk_bf16_f32 v235, v218, v222\n"
    "v_mfma_f32_16x16x32_bf16 v[168:171], a[248:251], v[120:123], v[168:171]\n"
    "v_cvt_pk_bf16_f32 v239, v219, v223\n"
    "v_mfma_f32_16x16x32_bf16 v[168:171], a[252:255], v[124:127], v[168:171]\n"
    "global_store_dwordx4 v10, v[224:227], s[14:15] offset:0 sc1\n"
    "s_waitcnt lgkmcnt(0)\n"
    "v_mfma_f32_16x16x32_bf16 v[172:175], a[224:227], v[128:131], v[172:175]\n"
    "v_mfma_f32_16x16x32_bf16 v[172:175], a[228:231], v[132:135], v[172:175]\n"
    "v_mfma_f32_16x16x32_bf16 v[172:175], a[232:235], v[136:139], v[172:175]\n"
    "v_mfma_f32_16x16x32_bf16 v[172:175], a[236:239], v[140:143], v[172:175]\n"
    "global_store_dwordx4 v10, v[228:231], s[14:15] offset:2048 sc1\n"
    "v_mfma_f32_16x16x32_bf16 v[172:175], a[240:243], v[144:147], v[172:175]\n"
    "v_mfma_f32_16x16x32_bf16 v[172:175], a[244:247], v[148:151], v[172:175]\n"
    "v_mfma_f32_16x16x32_bf16 v[172:175], a[248:251], v[152:155], v[172:175]\n"
    "v_mfma_f32_16x16x32_bf16 v[172:175], a[252:255], v[156:159], v[172:175]\n"
    "global_store_dwordx4 v11, v[232:235], s[14:15] offset:0 sc1\n"
    "s_waitcnt vmcnt(5)\n"
    "v_mfma_f32_16x16x32_bf16 v[160:163], v[32:35], a[0:3], 0\n"
    "v_mfma_f32_16x16x32_bf16 v[160:163], v[36:39], a[4:7], v[160:163]\n"
    "v_mfma_f32_16x16x32_bf16 v[160:163], v[40:43], a[8:11], v[160:163]\n"
    "v_cvt_pk_bf16_f32 v185, v168, v172\n"
    "v_cvt_pk_bf16_f32 v187, v169, v173\n"
    "v_mfma_f32_16x16x32_bf16 v[160:163], v[44:47], a[12:15], v[160:163]\n"
    "v_cvt_pk_bf16_f32 v189, v170, v174\n"
    "v_cvt_pk_bf16_f32 v191, v171, v175\n"
    "v_mfma_f32_16x16x32_bf16 v[160:163], v[48:51], a[16:19], v[160:163]\n"
    "global_store_dwordx4 v11, v[236:239], s[14:15] offset:2048 sc1\n"
    "v_mfma_f32_16x16x32_bf16 v[160:163], v[52:55], a[20:23], v[160:163]\n"
    "v_add_u32 v248, 131072, v248\n"
    "v_mfma_f32_16x16x32_bf16 v[160:163], v[56:59], a[24:27], v[160:163]\n"
    "v_mfma_f32_16x16x32_bf16 v[160:163], v[60:63], a[28:31], v[160:163]\n"
    "s_waitcnt vmcnt(4)\n"
    "v_mfma_f32_16x16x32_bf16 v[164:167], v[64:67], a[0:3], 0\n"
    "v_mfma_f32_16x16x32_bf16 v[164:167], v[68:71], a[4:7], v[164:167]\n"
    "global_load_dwordx4 v[96:99], v248, s[14:15] offset:-4096 sc1\n"
    "v_add_u32 v250, 8192, v248\n"
    "v_mfma_f32_16x16x32_bf16 v[164:167], v[72:75], a[8:11], v[164:167]\n"
    "v_mfma_f32_16x16x32_bf16 v[164:167], v[76:79], a[12:15], v[164:167]\n"
    "v_mfma_f32_16x16x32_bf16 v[164:167], v[80:83], a[16:19], v[164:167]\n"
    "ds_write_addtid_b32 v160 offset:0\n"
    "v_mfma_f32_16x16x32_bf16 v[164:167], v[84:87], a[20:23], v[164:167]\n"
    "ds_write_addtid_b32 v161 offset:288\n"
    "global_load_dwordx4 v[100:103], v248, s[14:15] offset:-3072 sc1\n"
    "v_mfma_f32_16x16x32_bf16 v[164:167], v[88:91], a[24:27], v[164:167]\n"
    "ds_write_addtid_b32 v162 offset:640\n"
    "v_mfma_f32_16x16x32_bf16 v[164:167], v[92:95], a[28:31], v[164:167]\n"
    "ds_write_addtid_b32 v163 offset:928\n"
    "v_mfma_f32_16x16x32_bf16 v[168:171], v[32:35], a[32:35], 0\n"
    "v_mfma_f32_16x16x32_bf16 v[168:171], v[36:39], a[36:39], v[168:171]\n"
    "global_load_dwordx4 v[128:131], v248, s[14:15] offset:-2048 sc1\n"
    "v_mfma_f32_16x16x32_bf16 v[168:171], v[40:43], a[40:43], v[168:171]\n"
    "ds_write_addtid_b32 v164 offset:1184\n"
    "v_mfma_f32_16x16x32_bf16 v[168:171], v[44:47], a[44:47], v[168:171]\n"
    "ds_write_addtid_b32 v165 offset:1472\n"
    "v_mfma_f32_16x16x32_bf16 v[168:171], v[48:51], a[48:51], v[168:171]\n"
    "ds_write_addtid_b32 v166 offset:1824\n"
    "v_mfma_f32_16x16x32_bf16 v[168:171], v[52:55], a[52:55], v[168:171]\n"
    "ds_write_addtid_b32 v167 offset:2112\n"
    "global_load_dwordx4 v[132:135], v248, s[14:15] offset:-1024 sc1\n"
    "v_mfma_f32_16x16x32_bf16 v[168:171], v[56:59], a[56:59], v[168:171]\n"
    "v_mfma_f32_16x16x32_bf16 v[168:171], v[60:63], a[60:63], v[168:171]\n"
    "v_mfma_f32_16x16x32_bf16 v[172:175], v[64:67], a[32:35], 0\n"
    "s_waitcnt lgkmcnt(0)\n"
    "v_mfma_f32_16x16x32_bf16 v[172:175], v[68:71], a[36:39], v[172:175]\n"
    "s_barrier\n"
    "v_mfma_f32_16x16x32_bf16 v[172:175], v[72:75], a[40:43], v[172:175]\n"
    "ds_read_b128 v[160:163], v13 offset:0\n"
    "global_load_dwordx4 v[104:107], v248, s[14:15] offset:2048 sc1\n"
    "v_mfma_f32_16x16x32_bf16 v[172:175], v[76:79], a[44:47], v[172:175]\n"
    "ds_read_b128 v[164:167], v13 offset:1184\n"
    "v_mfma_f32_16x16x32_bf16 v[172:175], v[80:83], a[48:51], v[172:175]\n"
    "ds_write_addtid_b32 v168 offset:2368\n"
    "v_mfma_f32_16x16x32_bf16 v[172:175], v[84:87], a[52:55], v[172:175]\n"
    "ds_write_addtid_b32 v169 offset:2656\n"
    "global_load_dwordx4 v[108:111], v248, s[14:15] offset:3072 sc1\n"
    "v_mfma_f32_16x16x32_bf16 v[172:175], v[88:91], a[56:59], v[172:175]\n"
    "ds_write_addtid_b32 v170 offset:3008\n"
    "v_mfma_f32_16x16x32_bf16 v[172:175], v[92:95], a[60:63], v[172:175]\n"
    "ds_write_addtid_b32 v171 offset:3296\n"
    "s_waitcnt lgkmcnt(5)\n"
    "v_mfma_f32_16x16x32_bf16 v[160:163], a[64:67], v[32:35], v[160:163]\n"
    "v_mfma_f32_16x16x32_bf16 v[160:163], a[68:71], v[36:39], v[160:163]\n"
    "global_load_dwordx4 v[136:139], v248, s[14:15] offset:0 sc1\n"
    "v_mfma_f32_16x16x32_bf16 v[160:163], a[72:75], v[40:43], v[160:163]\n"
    "ds_write_addtid_b32 v172 offset:3552\n"
    "v_mfma_f32_16x16x32_bf16 v[160:163], a[76:79], v[44:47], v[160:163]\n"
    "ds_write_addtid_b32 v173 offset:3840\n"
    "v_mfma_f32_16x16x32_bf16 v[160:163], a[80:83], v[48:51], v[160:163]\n"
    "ds_write_addtid_b32 v174 offset:4192\n"
    "v_mfma_f32_16x16x32_bf16 v[160:163], a[84:87], v[52:55], v[160:163]\n"
    "ds_write_addtid_b32 v175 offset:4480\n"
    "global_load_dwordx4 v[140:143], v248, s[14:15] offset:1024 sc1\n"
    "v_mfma_f32_16x16x32_bf16 v[160:163], a[88:91], v[56:59], v[160:163]\n"
    "v_mfma_f32_16x16x32_bf16 v[160:163], a[92:95], v[60:63], v[160:163]\n"
    "s_waitcnt lgkmcnt(8)\n"
    "v_mfma_f32_16x16x32_bf16 v[164:167], a[64:67], v[64:67], v[164:167]\n"
    "s_waitcnt vmcnt(8) lgkmcnt(0)\n"
    "v_mfma_f32_16x16x32_bf16 v[164:167], a[68:71], v[68:71], v[164:167]\n"
    "s_barrier\n"
    "v_mfma_f32_16x16x32_bf16 v[164:167], a[72:75], v[72:75], v[164:167]\n"
    "ds_read_b128 v[168:171], v13 offset:2368\n"
    "global_load_dwordx4 v[112:115], v250, s[14:15] offset:-4096 sc1\n"
    "v_mfma_f32_16x16x32_bf16 v[164:167], a[76:79], v[76:79], v[164:167]\n"
    "ds_read_b128 v[172:175], v13 offset:3552\n"
    "v_or_b32 v22, 3, v21\n"
    "v_mfma_f32_16x16x32_bf16 v[164:167], a[80:83], v[80:83], v[164:167]\n"
    "ds_write_addtid_b32 v160 offset:0\n"
    "v_mfma_f32_16x16x32_bf16 v[164:167], a[84:87], v[84:87], v[164:167]\n"
    "ds_write_addtid_b32 v161 offset:288\n"
    "global_load_dwordx4 v[116:119], v250, s[14:15] offset:-3072 sc1\n"
    "v_mfma_f32_16x16x32_bf16 v[164:167], a[88:91], v[88:91], v[164:167]\n"
    "ds_write_addtid_b32 v162 offset:640\n"
    "v_mfma_f32_16x16x32_bf16 v[164:167], a[92:95], v[92:95], v[164:167]\n"
    "ds_write_addtid_b32 v163 offset:928\n"
    "s_waitcnt lgkmcnt(5)\n"
    "v_mfma_f32_16x16x32_bf16 v[168:171], a[96:99], v[32:35], v[168:171]\n"
    "s_mov_b64 exec, s[66:67]\n"
    "global_store_dword v18, v22, s[14:15] sc1\n"
    "s_mov_b64 exec, -1\n"
    "v_mfma_f32_16x16x32_bf16 v[168:171], a[100:103], v[36:39], v[168:171]\n"
    "global_load_dwordx4 v[144:147], v250, s[14:15] offset:-2048 sc1\n"
    "v_mfma_f32_16x16x32_bf16 v[168:171], a[104:107], v[40:43], v[168:171]\n"
    "ds_write_addtid_b32 v164 offset:1184\n"
    "v_mfma_f32_16x16x32_bf16 v[168:171], a[108:111], v[44:47], v[168:171]\n"
    "ds_write_addtid_b32 v165 offset:1472\n"
    "v_mfma_f32_16x16x32_bf16 v[168:171], a[112:115], v[48:51], v[168:171]\n"
    "ds_write_addtid_b32 v166 offset:1824\n"
    "v_mfma_f32_16x16x32_bf16 v[168:171], a[116:119], v[52:55], v[168:171]\n"
    "ds_write_addtid_b32 v167 offset:2112\n"
    "global_load_dwordx4 v[148:151], v250, s[14:15] offset:-1024 sc1\n"
    "v_mfma_f32_16x16x32_bf16 v[168:171], a[120:123], v[56:59], v[168:171]\n"
    "v_mfma_f32_16x16x32_bf16 v[168:171], a[124:127], v[60:63], v[168:171]\n"
    "s_waitcnt lgkmcnt(8)\n"
    "v_mfma_f32_16x16x32_bf16 v[172:175], a[96:99], v[64:67], v[172:175]\n"
    "s_waitcnt lgkmcnt(0)\n"
    "v_mfma_f32_16x16x32_bf16 v[172:175], a[100:103], v[68:71], v[172:175]\n"
    "s_barrier\n"
    "v_mfma_f32_16x16x32_bf16 v[172:175], a[104:107], v[72:75], v[172:175]\n"
    "ds_read_b128 v[160:163], v13 offset:0\n"
    "global_load_dwordx4 v[120:123], v250, s[14:15] offset:2048 sc1\n"
    "v_mfma_f32_16x16x32_bf16 v[172:175], a[108:111], v[76:79], v[172:175]\n"
    "ds_read_b128 v[164:167], v13 offset:1184\n"
    "v_mfma_f32_16x16x32_bf16 v[172:175], a[112:115], v[80:83], v[172:175]\n"
    "ds_write_addtid_b32 v168 offset:2368\n"
    "v_mfma_f32_16x16x32_bf16 v[172:175], a[116:119], v[84:87], v[172:175]\n"
    "ds_write_addtid_b32 v169 offset:2656\n"
    "global_load_dwordx4 v[124:127], v250, s[14:15] offset:3072 sc1\n"
    "v_mfma_f32_16x16x32_bf16 v[172:175], a[120:123], v[88:91], v[172:175]\n"
    "ds_write_addtid_b32 v170 offset:3008\n"
    "v_mfma_f32_16x16x32_bf16 v[172:175], a[124:127], v[92:95], v[172:175]\n"
    "ds_write_addtid_b32 v171 offset:3296\n"
    "s_waitcnt lgkmcnt(5)\n"
    "v_mfma_f32_16x16x32_bf16 v[160:163], v[32:35], a[128:131], v[160:163]\n"
    "v_mfma_f32_16x16x32_bf16 v[160:163], v[36:39], a[132:135], v[160:163]\n"
    "global_load_dwordx4 v[152:155], v250, s[14:15] offset:0 sc1\n"
    "v_mfma_f32_16x16x32_bf16 v[160:163], v[40:43], a[136:139], v[160:163]\n"
    "ds_write_addtid_b32 v172 offset:3552\n"
    "v_mfma_f32_16x16x32_bf16 v[160:163], v[44:47], a[140:143], v[160:163]\n"
    "ds_write_addtid_b32 v173 offset:3840\n"
    "v_mfma_f32_16x16x32_bf16 v[160:163], v[48:51], a[144:147], v[160:163]\n"
    "ds_write_addtid_b32 v174 offset:4192\n"
    "v_mfma_f32_16x16x32_bf16 v[160:163], v[52:55], a[148:151], v[160:163]\n"
    "ds_write_addtid_b32 v175 offset:4480\n"
    "global_load_dwordx4 v[156:159], v250, s[14:15] offset:1024 sc1\n"
    "v_mfma_f32_16x16x32_bf16 v[160:163], v[56:59], a[152:155], v[160:163]\n"
    "v_mfma_f32_16x16x32_bf16 v[160:163], v[60:63], a[156:159], v[160:163]\n"
    "s_waitcnt lgkmcnt(8)\n"
    "v_mfma_f32_16x16x32_bf16 v[164:167], v[64:67], a[128:131], v[164:167]\n"
    "s_waitcnt lgkmcnt(0)\n"
    "v_mfma_f32_16x16x32_bf16 v[164:167], v[68:71], a[132:135], v[164:167]\n"
    "s_barrier\n"
    "v_mfma_f32_16x16x32_bf16 v[164:167], v[72:75], a[136:139], v[164:167]\n"
    "ds_read_b128 v[168:171], v13 offset:2368\n"
    "v_mfma_f32_16x16x32_bf16 v[164:167], v[76:79], a[140:143], v[164:167]\n"
    "ds_read_b128 v[172:175], v13 offset:3552\n"
    "v_mfma_f32_16x16x32_bf16 v[164:167], v[80:83], a[144:147], v[164:167]\n"
    "ds_write_addtid_b32 v160 offset:0\n"
    "v_mfma_f32_16x16x32_bf16 v[164:167], v[84:87], a[148:151], v[164:167]\n"
    "ds_write_addtid_b32 v161 offset:288\n"
    "v_mfma_f32_16x16x32_bf16 v[164:167], v[88:91], a[152:155], v[164:167]\n"
    "ds_write_addtid_b32 v162 offset:640\n"
    "v_mfma_f32_16x16x32_bf16 v[164:167], v[92:95], a[156:159], v[164:167]\n"
    "ds_write_addtid_b32 v163 offset:928\n"
    "s_waitcnt lgkmcnt(5)\n"
    "v_mfma_f32_16x16x32_bf16 v[168:171], v[32:35], a[160:163], v[168:171]\n"
    "v_mfma_f32_16x16x32_bf16 v[168:171], v[36:39], a[164:167], v[168:171]\n"
    "v_mfma_f32_16x16x32_bf16 v[168:171], v[40:43], a[168:171], v[168:171]\n"
    "ds_write_addtid_b32 v164 offset:1184\n"
    "v_mfma_f32_16x16x32_bf16 v[168:171], v[44:47], a[172:175], v[168:171]\n"
    "ds_write_addtid_b32 v165 offset:1472\n"
    "v_mfma_f32_16x16x32_bf16 v[168:171], v[48:51], a[176:179], v[168:171]\n"
    "ds_write_addtid_b32 v166 offset:1824\n"
    "v_mfma_f32_16x16x32_bf16 v[168:171], v[52:55], a[180:183], v[168:171]\n"
    "ds_write_addtid_b32 v167 offset:2112\n"
    "v_mfma_f32_16x16x32_bf16 v[168:171], v[56:59], a[184:187], v[168:171]\n"
    "v_mfma_f32_16x16x32_bf16 v[168:171], v[60:63], a[188:191], v[168:171]\n"
    "s_waitcnt lgkmcnt(8)\n"
    "v_mfma_f32_16x16x32_bf16 v[172:175], v[64:67], a[160:163], v[172:175]\n"
    "s_waitcnt lgkmcnt(0)\n"
    "v_mfma_f32_16x16x32_bf16 v[172:175], v[68:71], a[164:167], v[172:175]\n"
    "s_barrier\n"
    "v_mfma_f32_16x16x32_bf16 v[172:175], v[72:75], a[168:171], v[172:175]\n"
    "ds_read_b128 v[160:163], v13 offset:0\n"
    "v_mfma_f32_16x16x32_bf16 v[172:175], v[76:79], a[172:175], v[172:175]\n"
    "ds_read_b128 v[164:167], v13 offset:1184\n"
    "v_mfma_f32_16x16x32_bf16 v[172:175], v[80:83], a[176:179], v[172:175]\n"
    "ds_write_addtid_b32 v168 offset:2368\n"
    "v_mfma_f32_16x16x32_bf16 v[172:175], v[84:87], a[180:183], v[172:175]\n"
    "ds_write_addtid_b32 v169 offset:2656\n"
    "v_mfma_f32_16x16x32_bf16 v[172:175], v[88:91], a[184:187], v[172:175]\n"
    "ds_write_addtid_b32 v170 offset:3008\n"
    "v_mfma_f32_16x16x32_bf16 v[172:175], v[92:95], a[188:191], v[172:175]\n"
    "ds_write_addtid_b32 v171 offset:3296\n"
    "s_waitcnt lgkmcnt(5)\n"
    "v_mfma_f32_16x16x32_bf16 v[160:163], a[192:195], v[32:35], v[160:163]\n"
    "v_mfma_f32_16x16x32_bf16 v[160:163], a[196:199], v[36:39], v[160:163]\n"
    "v_mfma_f32_16x16x32_bf16 v[160:163], a[200:203], v[40:43], v[160:163]\n"
    "ds_write_addtid_b32 v172 offset:3552\n"
    "v_mfma_f32_16x16x32_bf16 v[160:163], a[204:207], v[44:47], v[160:163]\n"
    "ds_write_addtid_b32 v173 offset:3840\n"
    "v_mfma_f32_16x16x32_bf16 v[160:163], a[208:211], v[48:51], v[160:163]\n"
    "ds_write_addtid_b32 v174 offset:4192\n"
    "v_mfma_f32_16x16x32_bf16 v[160:163], a[212:215], v[52:55], v[160:163]\n"
    "ds_write_addtid_b32 v175 offset:4480\n"
    "v_mfma_f32_16x16x32_bf16 v[160:163], a[216:219], v[56:59], v[160:163]\n"
    "v_mfma_f32_16x16x32_bf16 v[160:163], a[220:223], v[60:63], v[160:163]\n"
    "s_waitcnt lgkmcnt(8)\n"
    "v_mfma_f32_16x16x32_bf16 v[164:167], a[192:195], v[64:67], v[164:167]\n"
    "s_waitcnt lgkmcnt(0)\n"
    "v_mfma_f32_16x16x32_bf16 v[164:167], a[196:199], v[68:71], v[164:167]\n"
    "s_barrier\n"
    "v_mfma_f32_16x16x32_bf16 v[164:167], a[200:203], v[72:75], v[164:167]\n"
    "ds_read_b128 v[168:171], v13 offset:2368\n"
    "v_mfma_f32_16x16x32_bf16 v[164:167], a[204:207], v[76:79], v[164:167]\n"
    "ds_read_b128 v[172:175], v13 offset:3552\n"
    "v_mfma_f32_16x16x32_bf16 v[164:167], a[208:211], v[80:83], v[164:167]\n"
    "v_mfma_f32_16x16x32_bf16 v[164:167], a[212:215], v[84:87], v[164:167]\n"
    "v_mfma_f32_16x16x32_bf16 v[164:167], a[216:219], v[88:91], v[164:167]\n"
    "v_mfma_f32_16x16x32_bf16 v[164:167], a[220:223], v[92:95], v[164:167]\n"
    "s_waitcnt lgkmcnt(1)\n"
    "v_mfma_f32_16x16x32_bf16 v[168:171], a[224:227], v[32:35], v[168:171]\n"
    "v_mfma_f32_16x16x32_bf16 v[168:171], a[228:231], v[36:39], v[168:171]\n"
    "v_mfma_f32_16x16x32_bf16 v[168:171], a[232:235], v[40:43], v[168:171]\n"
    "v_cvt_pk_bf16_f32 v192, v160, v164\n"
    "v_cvt_pk_bf16_f32 v194, v161, v165\n"
    "v_mfma_f32_16x16x32_bf16 v[168:171], a[236:239], v[44:47], v[168:171]\n"
    "v_cvt_pk_bf16_f32 v196, v162, v166\n"
    "v_cvt_pk_bf16_f32 v198, v163, v167\n"
    "v_mfma_f32_16x16x32_bf16 v[168:171], a[240:243], v[48:51], v[168:171]\n"
    "global_load_dword v20, v18, s[14:15] sc1\n"
    "v_mfma_f32_16x16x32_bf16 v[168:171], a[244:247], v[52:55], v[168:171]\n"
    "v_mfma_f32_16x16x32_bf16 v[168:171], a[248:251], v[56:59], v[168:171]\n"
    "v_mfma_f32_16x16x32_bf16 v[168:171], a[252:255], v[60:63], v[168:171]\n"
    "s_waitcnt lgkmcnt(0)\n"
    "v_mfma_f32_16x16x32_bf16 v[172:175], a[224:227], v[64:67], v[172:175]\n"
    "v_mfma_f32_16x16x32_bf16 v[172:175], a[228:231], v[68:71], v[172:175]\n"
    "v_mfma_f32_16x16x32_bf16 v[172:175], a[232:235], v[72:75], v[172:175]\n"
    "v_mfma_f32_16x16x32_bf16 v[172:175], a[236:239], v[76:79], v[172:175]\n"
    "v_mfma_f32_16x16x32_bf16 v[172:175], a[240:243], v[80:83], v[172:175]\n"
    "v_mfma_f32_16x16x32_bf16 v[172:175], a[244:247], v[84:87], v[172:175]\n"
    "v_mfma_f32_16x16x32_bf16 v[172:175], a[248:251], v[88:91], v[172:175]\n"
    "v_mfma_f32_16x16x32_bf16 v[172:175], a[252:255], v[92:95], v[172:175]\n"
    "s_waitcnt vmcnt(3)\n"
    "v_mfma_f32_16x16x32_bf16 v[160:163], v[96:99], a[0:3], 0\n"
    "v_mfma_f32_16x16x32_bf16 v[160:163], v[100:103], a[4:7], v[160:163]\n"
    "v_mfma_f32_16x16x32_bf16 v[160:163], v[104:107], a[8:11], v[160:163]\n"
    "v_cvt_pk_bf16_f32 v200, v168, v172\n"
    "v_cvt_pk_bf16_f32 v202, v169, v173\n"
    "v_mfma_f32_16x16x32_bf16 v[160:163], v[108:111], a[12:15], v[160:163]\n"
    "v_cvt_pk_bf16_f32 v204, v170, v174\n"
    "v_cvt_pk_bf16_f32 v206, v171, v175\n"
    "v_mfma_f32_16x16x32_bf16 v[160:163], v[112:115], a[16:19], v[160:163]\n"
    "v_mfma_f32_16x16x32_bf16 v[160:163], v[116:119], a[20:23], v[160:163]\n"
    "v_mfma_f32_16x16x32_bf16 v[160:163], v[120:123], a[24:27], v[160:163]\n"
    "v_mfma_f32_16x16x32_bf16 v[160:163], v[124:127], a[28:31], v[160:163]\n"
    "s_waitcnt vmcnt(1)\n"
    "v_mfma_f32_16x16x32_bf16 v[164:167], v[128:131], a[0:3], 0\n"
    "v_mfma_f32_16x16x32_bf16 v[164:167], v[132:135], a[4:7], v[164:167]\n"
    "v_mfma_f32_16x16x32_bf16 v[164:167], v[136:139], a[8:11], v[164:167]\n"
    "v_mfma_f32_16x16x32_bf16 v[164:167], v[140:143], a[12:15], v[164:167]\n"
    "v_mfma_f32_16x16x32_bf16 v[164:167], v[144:147], a[16:19], v[164:167]\n"
    "ds_write_addtid_b32 v160 offset:0\n"
    "v_mfma_f32_16x16x32_bf16 v[164:167], v[148:151], a[20:23], v[164:167]\n"
    "ds_write_addtid_b32 v161 offset:288\n"
    "v_mfma_f32_16x16x32_bf16 v[164:167], v[152:155], a[24:27], v[164:167]\n"
    "ds_write_addtid_b32 v162 offset:640\n"
    "v_mfma_f32_16x16x32_bf16 v[164:167], v[156:159], a[28:31], v[164:167]\n"
    "ds_write_addtid_b32 v163 offset:928\n"
    "v_mfma_f32_16x16x32_bf16 v[168:171], v[96:99], a[32:35], 0\n"
    "s_waitcnt vmcnt(0)\n"
    "v_cmp_eq_u32_e64 s[68:69], v20, v19\n"
    "v_mfma_f32_16x16x32_bf16 v[168:171], v[100:103], a[36:39], v[168:171]\n"
    "v_and_b32 v22, 15, v20\n"
    "v_cmp_gt_u32_e64 s[70:71], 3, v22\n"
    "v_mfma_f32_16x16x32_bf16 v[168:171], v[104:107], a[40:43], v[168:171]\n"
    "ds_write_addtid_b32 v164 offset:1184\n"
    "s_or_b32 s68, s68, s70\n"
    "v_mfma_f32_16x16x32_bf16 v[168:171], v[108:111], a[44:47], v[168:171]\n"
    "ds_write_addtid_b32 v165 offset:1472\n"
    "v_mfma_f32_16x16x32_bf16 v[168:171], v[112:115], a[48:51], v[168:171]\n"
    "ds_write_addtid_b32 v166 offset:1824\n"
    "v_mfma_f32_16x16x32_bf16 v[168:171], v[116:119], a[52:55], v[168:171]\n"
    "ds_write_addtid_b32 v167 offset:2112\n"
    "v_add_u32 v248, 131072, v248\n"
    "v_mfma_f32_16x16x32_bf16 v[168:171], v[120:123], a[56:59], v[168:171]\n"
    "s_and_b32 s68, s68, 15\n"
    "s_cbranch_scc1 POLL2_%=\n"
    "BACK2_%=:\n"
    "v_mfma_f32_16x16x32_bf16 v[168:171], v[124:127], a[60:63], v[168:171]\n"
    "v_mfma_f32_16x16x32_bf16 v[172:175], v[128:131], a[32:35], 0\n"
    "s_waitcnt lgkmcnt(0)\n"
    "v_mfma_f32_16x16x32_bf16 v[172:175], v[132:135], a[36:39], v[172:175]\n"
    "s_barrier\n"
    "v_mfma_f32_16x16x32_bf16 v[172:175], v[136:139], a[40:43], v[172:175]\n"
    "ds_read_b128 v[160:163], v13 offset:0\n"
    "global_load_dwordx4 v[32:35], v248, s[14:15] offset:-4096 sc1\n"
    "v_mfma_f32_16x16x32_bf16 v[172:175], v[140:143], a[44:47], v[172:175]\n"
    "ds_read_b128 v[164:167], v13 offset:1184\n"
    "v_add_u32 v250, 8192, v248\n"
    "v_mfma_f32_16x16x32_bf16 v[172:175], v[144:147], a[48:51], v[172:175]\n"
    "ds_write_addtid_b32 v168 offset:2368\n"
    "v_mfma_f32_16x16x32_bf16 v[172:175], v[148:151], a[52:55], v[172:175]\n"
    "ds_write_addtid_b32 v169 offset:2656\n"
    "global_load_dwordx4 v[36:39], v248, s[14:15] offset:-3072 sc1\n"
    "v_mfma_f32_16x16x32_bf16 v[172:175], v[152:155], a[56:59], v[172:175]\n"
    "ds_write_addtid_b32 v170 offset:3008\n"
    "v_mfma_f32_16x16x32_bf16 v[172:175], v[156:159], a[60:63], v[172:175]\n"
    "ds_write_addtid_b32 v171 offset:3296\n"
    "s_waitcnt lgkmcnt(5)\n"
    "v_mfma_f32_16x16x32_bf16 v[160:163], a[64:67], v[96:99], v[160:163]\n"
    "v_mfma_f32_16x16x32_bf16 v[160:163], a[68:71], v[100:103], v[160:163]\n"
    "global_load_dwordx4 v[64:67], v248, s[14:15] offset:-2048 sc1\n"
    "v_mfma_f32_16x16x32_bf16 v[160:163], a[72:75], v[104:107], v[160:163]\n"
    "ds_write_addtid_b32 v172 offset:3552\n"
    "v_mfma_f32_16x16x32_bf16 v[160:163], a[76:79], v[108:111], v[160:163]\n"
    "ds_write_addtid_b32 v173 offset:3840\n"
    "v_mfma_f32_16x16x32_bf16 v[160:163], a[80:83], v[112:115], v[160:163]\n"
    "ds_write_addtid_b32 v174 offset:4192\n"
    "v_mfma_f32_16x16x32_bf16 v[160:163], a[84:87], v[116:119], v[160:163]\n"
    "ds_write_addtid_b32 v175 offset:4480\n"
    "global_load_dwordx4 v[68:71], v248, s[14:15] offset:-1024 sc1\n"
    "v_mfma_f32_16x16x32_bf16 v[160:163], a[88:91], v[120:123], v[160:163]\n"
    "v_mfma_f32_16x16x32_bf16 v[160:163], a[92:95], v[124:127], v[160:163]\n"
    "s_waitcnt lgkmcnt(8)\n"
    "v_mfma_f32_16x16x32_bf16 v[164:167], a[64:67], v[128:131], v[164:167]\n"
    "s_waitcnt lgkmcnt(0)\n"
    "v_mfma_f32_16x16x32_bf16 v[164:167], a[68:71], v[132:135], v[164:167]\n"
    "s_barrier\n"
    "v_mfma_f32_16x16x32_bf16 v[164:167], a[72:75], v[136:139], v[164:167]\n"
    "ds_read_b128 v[168:171], v13 offset:2368\n"
    "global_load_dwordx4 v[40:43], v248, s[14:15] offset:0 sc1\n"
    "v_mfma_f32_16x16x32_bf16 v[164:167], a[76:79], v[140:143], v[164:167]\n"
    "ds_read_b128 v[172:175], v13 offset:3552\n"
    "v_mfma_f32_16x16x32_bf16 v[164:167], a[80:83], v[144:147], v[164:167]\n"
    "ds_write_addtid_b32 v160 offset:0\n"
    "v_mfma_f32_16x16x32_bf16 v[164:167], a[84:87], v[148:151], v[164:167]\n"
    "ds_write_addtid_b32 v161 offset:288\n"
    "global_load_dwordx4 v[44:47], v248, s[14:15] offset:1024 sc1\n"
    "v_mfma_f32_16x16x32_bf16 v[164:167], a[88:91], v[152:155], v[164:167]\n"
    "ds_write_addtid_b32 v162 offset:640\n"
    "v_mfma_f32_16x16x32_bf16 v[164:167], a[92:95], v[156:159], v[164:167]\n"
    "ds_write_addtid_b32 v163 offset:928\n"
    "s_waitcnt lgkmcnt(5)\n"
    "v_mfma_f32_16x16x32_bf16 v[168:171], a[96:99], v[96:99], v[168:171]\n"
    "v_mfma_f32_16x16x32_bf16 v[168:171], a[100:103], v[100:103], v[168:171]\n"
    "global_load_dwordx4 v[72:75], v248, s[14:15] offset:2048 sc1\n"
    "v_mfma_f32_16x16x32_bf16 v[168:171], a[104:107], v[104:107], v[168:171]\n"
    "ds_write_addtid_b32 v164 offset:1184\n"
    "v_mfma_f32_16x16x32_bf16 v[168:171], a[108:111], v[108:111], v[168:171]\n"
    "ds_write_addtid_b32 v165 offset:1472\n"
    "v_mfma_f32_16x16x32_bf16 v[168:171], a[112:115], v[112:115], v[168:171]\n"
    "ds_write_addtid_b32 v166 offset:1824\n"
    "v_mfma_f32_16x16x32_bf16 v[168:171], a[116:119], v[116:119], v[168:171]\n"
    "ds_write_addtid_b32 v167 offset:2112\n"
    "global_load_dwordx4 v[76:79], v248, s[14:15] offset:3072 sc1\n"
    "v_mfma_f32_16x16x32_bf16 v[168:171], a[120:123], v[120:123], v[168:171]\n"
    "v_mfma_f32_16x16x32_bf16 v[168:171], a[124:127], v[124:127], v[168:171]\n"
    "s_waitcnt lgkmcnt(8)\n"
    "v_mfma_f32_16x16x32_bf16 v[172:175], a[96:99], v[128:131], v[172:175]\n"
    "s_waitcnt lgkmcnt(0)\n"
    "v_mfma_f32_16x16x32_bf16 v[172:175], a[100:103], v[132:135], v[172:175]\n"
    "s_barrier\n"
    "v_mfma_f32_16x16x32_bf16 v[172:175], a[104:107], v[136:139], v[172:175]\n"
    "ds_read_b128 v[160:163], v13 offset:0\n"
    "global_load_dwordx4 v[48:51], v250, s[14:15] offset:-4096 sc1\n"
    "v_mfma_f32_16x16x32_bf16 v[172:175], a[108:111], v[140:143], v[172:175]\n"
    "ds_read_b128 v[164:167], v13 offset:1184\n"
    "v_mfma_f32_16x16x32_bf16 v[172:175], a[112:115], v[144:147], v[172:175]\n"
    "ds_write_addtid_b32 v168 offset:2368\n"
    "v_mfma_f32_16x16x32_bf16 v[172:175], a[116:119], v[148:151], v[172:175]\n"
    "ds_write_addtid_b32 v169 offset:2656\n"
    "global_load_dwordx4 v[52:55], v250, s[14:15] offset:-3072 sc1\n"
    "v_mfma_f32_16x16x32_bf16 v[172:175], a[120:123], v[152:155], v[172:175]\n"
    "ds_write_addtid_b32 v170 offset:3008\n"
    "v_mfma_f32_16x16x32_bf16 v[172:175], a[124:127], v[156:159], v[172:175]\n"
    "ds_write_addtid_b32 v171 offset:3296\n"
    "s_waitcnt lgkmcnt(5)\n"
    "v_mfma_f32_16x16x32_bf16 v[160:163], v[96:99], a[128:131], v[160:163]\n"
    "v_mfma_f32_16x16x32_bf16 v[160:163], v[100:103], a[132:135], v[160:163]\n"
    "global_load_dwordx4 v[80:83], v250, s[14:15] offset:-2048 sc1\n"
    "v_mfma_f32_16x16x32_bf16 v[160:163], v[104:107], a[136:139], v[160:163]\n"
    "ds_write_addtid_b32 v172 offset:3552\n"
    "v_mfma_f32_16x16x32_bf16 v[160:163], v[108:111], a[140:143], v[160:163]\n"
    "ds_write_addtid_b32 v173 offset:3840\n"
    "v_mfma_f32_16x16x32_bf16 v[160:163], v[112:115], a[144:147], v[160:163]\n"
    "ds_write_addtid_b32 v174 offset:4192\n"
    "v_mfma_f32_16x16x32_bf16 v[160:163], v[116:119], a[148:151], v[160:163]\n"
    "ds_write_addtid_b32 v175 offset:4480\n"
    "global_load_dwordx4 v[84:87], v250, s[14:15] offset:-1024 sc1\n"
    "v_mfma_f32_16x16x32_bf16 v[160:163], v[120:123], a[152:155], v[160:163]\n"
    "v_mfma_f32_16x16x32_bf16 v[160:163], v[124:127], a[156:159], v[160:163]\n"
    "s_waitcnt lgkmcnt(8)\n"
    "v_mfma_f32_16x16x32_bf16 v[164:167], v[128:131], a[128:131], v[164:167]\n"
    "s_waitcnt lgkmcnt(0)\n"
    "v_mfma_f32_16x16x32_bf16 v[164:167], v[132:135], a[132:135], v[164:167]\n"
    "s_barrier\n"
    "v_mfma_f32_16x16x32_bf16 v[164:167], v[136:139], a[136:139], v[164:167]\n"
    "ds_read_b128 v[168:171], v13 offset:2368\n"
    "global_load_dwordx4 v[56:59], v250, s[14:15] offset:0 sc1\n"
    "v_mfma_f32_16x16x32_bf16 v[164:167], v[140:143], a[140:143], v[164:167]\n"
    "ds_read_b128 v[172:175], v13 offset:3552\n"
    "v_mfma_f32_16x16x32_bf16 v[164:167], v[144:147], a[144:147], v[164:167]\n"
    "ds_write_addtid_b32 v160 offset:0\n"
    "v_mfma_f32_16x16x32_bf16 v[164:167], v[148:151], a[148:151], v[164:167]\n"
    "ds_write_addtid_b32 v161 offset:288\n"
    "global_load_dwordx4 v[60:63], v250, s[14:15] offset:1024 sc1\n"
    "v_mfma_f32_16x16x32_bf16 v[164:167], v[152:155], a[152:155], v[164:167]\n"
    "ds_write_addtid_b32 v162 offset:640\n"
    "v_mfma_f32_16x16x32_bf16 v[164:167], v[156:159], a[156:159], v[164:167]\n"
    "ds_write_addtid_b32 v163 offset:928\n"
    "s_waitcnt lgkmcnt(5)\n"
    "v_mfma_f32_16x16x32_bf16 v[168:171], v[96:99], a[160:163], v[168:171]\n"
    "v_mfma_f32_16x16x32_bf16 v[168:171], v[100:103], a[164:167], v[168:171]\n"
    "global_load_dwordx4 v[88:91], v250, s[14:15] offset:2048 sc1\n"
    "v_mfma_f32_16x16x32_bf16 v[168:171], v[104:107], a[168:171], v[168:171]\n"
    "ds_write_addtid_b32 v164 offset:1184\n"
    "v_mfma_f32_16x16x32_bf16 v[168:171], v[108:111], a[172:175], v[168:171]\n"
    "ds_write_addtid_b32 v165 offset:1472\n"
    "v_mfma_f32_16x16x32_bf16 v[168:171], v[112:115], a[176:179], v[168:171]\n"
    "ds_write_addtid_b32 v166 offset:1824\n"
    "v_mfma_f32_16x16x32_bf16 v[168:171], v[116:119], a[180:183], v[168:171]\n"
    "ds_write_addtid_b32 v167 offset:2112\n"
    "global_load_dwordx4 v[92:95], v250, s[14:15] offset:3072 sc1\n"
    "v_mfma_f32_16x16x32_bf16 v[168:171], v[120:123], a[184:187], v[168:171]\n"
    "v_mfma_f32_16x16x32_bf16 v[168:171], v[124:127], a[188:191], v[168:171]\n"
    "s_waitcnt lgkmcnt(8)\n"
    "v_mfma_f32_16x16x32_bf16 v[172:175], v[128:131], a[160:163], v[172:175]\n"
    "s_waitcnt lgkmcnt(0)\n"
    "v_mfma_f32_16x16x32_bf16 v[172:175], v[132:135], a[164:167], v[172:175]\n"
    "s_barrier\n"
    "v_mfma_f32_16x16x32_bf16 v[172:175], v[136:139], a[168:171], v[172:175]\n"
    "ds_read_b128 v[160:163], v13 offset:0\n"
    "v_mfma_f32_16x16x32_bf16 v[172:175], v[140:143], a[172:175], v[172:175]\n"
    "ds_read_b128 v[164:167], v13 offset:1184\n"
    "v_mfma_f32_16x16x32_bf16 v[172:175], v[144:147], a[176:179], v[172:175]\n"
    "ds_write_addtid_b32 v168 offset:2368\n"
    "v_mfma_f32_16x16x32_bf16 v[172:175], v[148:151], a[180:183], v[172:175]\n"
    "ds_write_addtid_b32 v169 offset:2656\n"
    "v_mfma_f32_16x16x32_bf16 v[172:175], v[152:155], a[184:187], v[172:175]\n"
    "ds_write_addtid_b32 v170 offset:3008\n"
    "v_mfma_f32_16x16x32_bf16 v[172:175], v[156:159], a[188:191], v[172:175]\n"
    "ds_write_addtid_b32 v171 offset:3296\n"
    "s_waitcnt lgkmcnt(5)\n"
    "v_mfma_f32_16x16x32_bf16 v[160:163], a[192:195], v[96:99], v[160:163]\n"
    "global_store_dwordx2 v14, v[176:177], s[52:53] offset:0 sc0 sc1 nt\n"
    "v_mfma_f32_16x16x32_bf16 v[160:163], a[196:199], v[100:103], v[160:163]\n"
    "v_mfma_f32_16x16x32_bf16 v[160:163], a[200:203], v[104:107], v[160:163]\n"
    "ds_write_addtid_b32 v172 offset:3552\n"
    "v_mfma_f32_16x16x32_bf16 v[160:163], a[204:207], v[108:111], v[160:163]\n"
    "ds_write_addtid_b32 v173 offset:3840\n"
    "v_mfma_f32_16x16x32_bf16 v[160:163], a[208:211], v[112:115], v[160:163]\n"
    "ds_write_addtid_b32 v174 offset:4192\n"
    "global_store_dwordx2 v14, v[178:179], s[54:55] offset:0 sc0 sc1 nt\n"
    "v_mfma_f32_16x16x32_bf16 v[160:163], a[212:215], v[116:119], v[160:163]\n"
    "ds_write_addtid_b32 v175 offset:4480\n"
    "v_mfma_f32_16x16x32_bf16 v[160:163], a[216:219], v[120:123], v[160:163]\n"
    "v_mfma_f32_16x16x32_bf16 v[160:163], a[220:223], v[124:127], v[160:163]\n"
    "s_waitcnt lgkmcnt(8)\n"
    "v_mfma_f32_16x16x32_bf16 v[164:167], a[192:195], v[128:131], v[164:167]\n"
    "global_store_dwordx2 v14, v[180:181], s[56:57] offset:0 sc0 sc1 nt\n"
    "s_waitcnt lgkmcnt(0)\n"
    "v_mfma_f32_16x16x32_bf16 v[164:167], a[196:199], v[132:135], v[164:167]\n"
    "s_barrier\n"
    "v_mfma_f32_16x16x32_bf16 v[164:167], a[200:203], v[136:139], v[164:167]\n"
    "ds_read_b128 v[168:171], v13 offset:2368\n"
    "v_mfma_f32_16x16x32_bf16 v[164:167], a[204:207], v[140:143], v[164:167]\n"
    "ds_read_b128 v[172:175], v13 offset:3552\n"
    "v_mfma_f32_16x16x32_bf16 v[164:167], a[208:211], v[144:147], v[164:167]\n"
    "global_store_dwordx2 v14, v[182:183], s[58:59] offset:0 sc0 sc1 nt\n"
    "v_mfma_f32_16x16x32_bf16 v[164:167], a[212:215], v[148:151], v[164:167]\n"
    "v_mfma_f32_16x16x32_bf16 v[164:167], a[216:219], v[152:155], v[164:167]\n"
    "v_mfma_f32_16x16x32_bf16 v[164:167], a[220:223], v[156:159], v[164:167]\n"
    "s_waitcnt lgkmcnt(1)\n"
    "v_mfma_f32_16x16x32_bf16 v[168:171], a[224:227], v[96:99], v[168:171]\n"
    "global_store_dwordx2 v15, v[184:185], s[52:53] offset:0 sc0 sc1 nt\n"
    "v_mfma_f32_16x16x32_bf16 v[168:171], a[228:231], v[100:103], v[168:171]\n"
    "v_mfma_f32_16x16x32_bf16 v[168:171], a[232:235], v[104:107], v[168:171]\n"
    "v_cvt_pk_bf16_f32 v193, v160, v164\n"
    "v_cvt_pk_bf16_f32 v195, v161, v165\n"
    "v_mfma_f32_16x16x32_bf16 v[168:171], a[236:239], v[108:111], v[168:171]\n"
    "v_cvt_pk_bf16_f32 v197, v162, v166\n"
    "v_cvt_pk_bf16_f32 v199, v163, v167\n"
    "v_mfma_f32_16x16x32_bf16 v[168:171], a[240:243], v[112:115], v[168:171]\n"
    "global_store_dwordx2 v15, v[186:187], s[54:55] offset:0 sc0 sc1 nt\n"
    "v_mfma_f32_16x16x32_bf16 v[168:171], a[244:247], v[116:119], v[168:171]\n"
    "v_mfma_f32_16x16x32_bf16 v[168:171], a[248:251], v[120:123], v[168:171]\n"
    "v_mfma_f32_16x16x32_bf16 v[168:171], a[252:255], v[124:127], v[168:171]\n"
    "s_waitcnt lgkmcnt(0)\n"
    "v_mfma_f32_16x16x32_bf16 v[172:175], a[224:227], v[128:131], v[172:175]\n"
    "global_store_dwordx2 v15, v[188:189], s[56:57] offset:0 sc0 sc1 nt\n"
    "v_mfma_f32_16x16x32_bf16 v[172:175], a[228:231], v[132:135], v[172:175]\n"
    "v_mfma_f32_16x16x32_bf16 v[172:175], a[232:235], v[136:139], v[172:175]\n"
    "v_mfma_f32_16x16x32_bf16 v[172:175], a[236:239], v[140:143], v[172:175]\n"
    "v_mfma_f32_16x16x32_bf16 v[172:175], a[240:243], v[144:147], v[172:175]\n"
    "global_store_dwordx2 v15, v[190:191], s[58:59] offset:0 sc0 sc1 nt\n"
    "v_mfma_f32_16x16x32_bf16 v[172:175], a[244:247], v[148:151], v[172:175]\n"
    "v_mfma_f32_16x16x32_bf16 v[172:175], a[248:251], v[152:155], v[172:175]\n"
    "v_mfma_f32_16x16x32_bf16 v[172:175], a[252:255], v[156:159], v[172:175]\n"
    "s_waitcnt vmcnt(10)\n"
    "v_mfma_f32_16x16x32_bf16 v[160:163], v[32:35], a[0:3], 0\n"
    "v_mfma_f32_16x16x32_bf16 v[160:163], v[36:39], a[4:7], v[160:163]\n"
    "v_mfma_f32_16x16x32_bf16 v[160:163], v[40:43], a[8:11], v[160:163]\n"
    "v_cvt_pk_bf16_f32 v201, v168, v172\n"
    "v_cvt_pk_bf16_f32 v203, v169, v173\n"
    "v_mfma_f32_16x16x32_bf16 v[160:163], v[44:47], a[12:15], v[160:163]\n"
    "v_cvt_pk_bf16_f32 v205, v170, v174\n"
    "v_cvt_pk_bf16_f32 v207, v171, v175\n"
    "v_mfma_f32_16x16x32_bf16 v[160:163], v[48:51], a[16:19], v[160:163]\n"
    "v_mfma_f32_16x16x32_bf16 v[160:163], v[52:55], a[20:23], v[160:163]\n"
    "v_add_u32 v248, 131072, v248\n"
    "v_mfma_f32_16x16x32_bf16 v[160:163], v[56:59], a[24:27], v[160:163]\n"
    "v_mfma_f32_16x16x32_bf16 v[160:163], v[60:63], a[28:31], v[160:163]\n"
    "s_waitcnt vmcnt(8)\n"
    "v_mfma_f32_16x16x32_bf16 v[164:167], v[64:67], a[0:3], 0\n"
    "v_mfma_f32_16x16x32_bf16 v[164:167], v[68:71], a[4:7], v[164:167]\n"
    "global_load_dwordx4 v[96:99], v248, s[14:15] offset:-4096 sc1\n"
    "v_add_u32 v250, 8192, v248\n"
    "v_mfma_f32_16x16x32_bf16 v[164:167], v[72:75], a[8:11], v[164:167]\n"
    "HOP2_%=:\n"
    "s_cbranch_vccnz HOP3_%=\n"
    "v_mfma_f32_16x16x32_bf16 v[164:167], v[76:79], a[12:15], v[164:167]\n"
    "v_mfma_f32_16x16x32_bf16 v[164:167], v[80:83], a[16:19], v[164:167]\n"
    "ds_write_addtid_b32 v160 offset:0\n"
    "v_mfma_f32_16x16x32_bf16 v[164:167], v[84:87], a[20:23], v[164:167]\n"
    "ds_write_addtid_b32 v161 offset:288\n"
    "global_load_dwordx4 v[100:103], v248, s[14:15] offset:-3072 sc1\n"
    "v_mfma_f32_16x16x32_bf16 v[164:167], v[88:91], a[24:27], v[164:167]\n"
    "ds_write_addtid_b32 v162 offset:640\n"
    "v_mfma_f32_16x16x32_bf16 v[164:167], v[92:95], a[28:31], v[164:167]\n"
    "ds_write_addtid_b32 v163 offset:928\n"
    "v_mfma_f32_16x16x32_bf16 v[168:171], v[32:35], a[32:35], 0\n"
    "v_mfma_f32_16x16x32_bf16 v[168:171], v[36:39], a[36:39], v[168:171]\n"
    "global_load_dwordx4 v[128:131], v248, s[14:15] offset:-2048 sc1\n"
    "v_mfma_f32_16x16x32_bf16 v[168:171], v[40:43], a[40:43], v[168:171]\n"
    "ds_write_addtid_b32 v164 offset:1184\n"
    "v_mfma_f32_16x16x32_bf16 v[168:171], v[44:47], a[44:47], v[168:171]\n"
    "ds_write_addtid_b32 v165 offset:1472\n"
    "v_mfma_f32_16x16x32_bf16 v[168:171], v[48:51], a[48:51], v[168:171]\n"
    "ds_write_addtid_b32 v166 offset:1824\n"
    "v_mfma_f32_16x16x32_bf16 v[168:171], v[52:55], a[52:55], v[168:171]\n"
    "ds_write_addtid_b32 v167 offset:2112\n"
    "global_load_dwordx4 v[132:135], v248, s[14:15] offset:-1024 sc1\n"
    "v_mfma_f32_16x16x32_bf16 v[168:171], v[56:59], a[56:59], v[168:171]\n"
    "v_mfma_f32_16x16x32_bf16 v[168:171], v[60:63], a[60:63], v[168:171]\n"
    "v_mfma_f32_16x16x32_bf16 v[172:175], v[64:67], a[32:35], 0\n"
    "s_waitcnt lgkmcnt(0)\n"
    "v_mfma_f32_16x16x32_bf16 v[172:175], v[68:71], a[36:39], v[172:175]\n"
    "s_barrier\n"
    "v_mfma_f32_16x16x32_bf16 v[172:175], v[72:75], a[40:43], v[172:175]\n"
    "ds_read_b128 v[160:163], v13 offset:0\n"
    "global_load_dwordx4 v[104:107], v248, s[14:15] offset:0 sc1\n"
    "v_mfma_f32_16x16x32_bf16 v[172:175], v[76:79], a[44:47], v[172:175]\n"
    "ds_read_b128 v[164:167], v13 offset:1184\n"
    "v_mfma_f32_16x16x32_bf16 v[172:175], v[80:83], a[48:51], v[172:175]\n"
    "ds_write_addtid_b32 v168 offset:2368\n"
    "v_mfma_f32_16x16x32_bf16 v[172:175], v[84:87], a[52:55], v[172:175]\n"
    "ds_write_addtid_b32 v169 offset:2656\n"
    "global_load_dwordx4 v[108:111], v248, s[14:15] offset:1024 sc1\n"
    "v_mfma_f32_16x16x32_bf16 v[172:175], v[88:91], a[56:59], v[172:175]\n"
    "ds_write_addtid_b32 v170 offset:3008\n"
    "v_mfma_f32_16x16x32_bf16 v[172:175], v[92:95], a[60:63], v[172:175]\n"
    "ds_write_addtid_b32 v171 offset:3296\n"
    "s_waitcnt lgkmcnt(5)\n"
    "v_mfma_f32_16x16x32_bf16 v[160:163], a[64:67], v[32:35], v[160:163]\n"
    "v_mfma_f32_16x16x32_bf16 v[160:163], a[68:71], v[36:39], v[160:163]\n"
    "global_load_dwordx4 v[136:139], v248, s[14:15] offset:2048 sc1\n"
    "v_mfma_f32_16x16x32_bf16 v[160:163], a[72:75], v[40:43], v[160:163]\n"
    "ds_write_addtid_b32 v172 offset:3552\n"
    "v_mfma_f32_16x16x32_bf16 v[160:163], a[76:79], v[44:47], v[160:163]\n"
    "ds_write_addtid_b32 v173 offset:3840\n"
    "v_mfma_f32_16x16x32_bf16 v[160:163], a[80:83], v[48:51], v[160:163]\n"
    "ds_write_addtid_b32 v174 offset:4192\n"
    "v_mfma_f32_16x16x32_bf16 v[160:163], a[84:87], v[52:55], v[160:163]\n"
    "ds_write_addtid_b32 v175 offset:4480\n"
    "global_load_dwordx4 v[140:143], v248, s[14:15] offset:3072 sc1\n"
    "v_mfma_f32_16x16x32_bf16 v[160:163], a[88:91], v[56:59], v[160:163]\n"
    "v_mfma_f32_16x16x32_bf16 v[160:163], a[92:95], v[60:63], v[160:163]\n"
    "s_waitcnt lgkmcnt(8)\n"
    "v_mfma_f32_16x16x32_bf16 v[164:167], a[64:67], v[64:67], v[164:167]\n"
    "s_waitcnt lgkmcnt(0)\n"
    "v_mfma_f32_16x16x32_bf16 v[164:167], a[68:71], v[68:71], v[164:167]\n"
    "s_barrier\n"
    "v_mfma_f32_16x16x32_bf16 v[164:167], a[72:75], v[72:75], v[164:167]\n"
    "ds_read_b128 v[168:171], v13 offset:2368\n"
    "global_load_dwordx4 v[112:115], v250, s[14:15] offset:-4096 sc1\n"
    "v_mfma_f32_16x16x32_bf16 v[164:167], a[76:79], v[76:79], v[164:167]\n"
    "ds_read_b128 v[172:175], v13 offset:3552\n"
    "v_mfma_f32_16x16x32_bf16 v[164:167], a[80:83], v[80:83], v[164:167]\n"
    "ds_write_addtid_b32 v160 offset:0\n"
    "v_mfma_f32_16x16x32_bf16 v[164:167], a[84:87], v[84:87], v[164:167]\n"
    "ds_write_addtid_b32 v161 offset:288\n"
    "global_load_dwordx4 v[116:119], v250, s[14:15] offset:-3072 sc1\n"
    "v_mfma_f32_16x16x32_bf16 v[164:167], a[88:91], v[88:91], v[164:167]\n"
    "ds_write_addtid_b32 v162 offset:640\n"
    "v_mfma_f32_16x16x32_bf16 v[164:167], a[92:95], v[92:95], v[164:167]\n"
    "ds_write_addtid_b32 v163 offset:928\n"
    "s_waitcnt lgkmcnt(5)\n"
    "v_mfma_f32_16x16x32_bf16 v[168:171], a[96:99], v[32:35], v[168:171]\n"
    "v_mfma_f32_16x16x32_bf16 v[168:171], a[100:103], v[36:39], v[168:171]\n"
    "global_load_dwordx4 v[144:147], v250, s[14:15] offset:-2048 sc1\n"
    "v_mfma_f32_16x16x32_bf16 v[168:171], a[104:107], v[40:43], v[168:171]\n"
    "ds_write_addtid_b32 v164 offset:1184\n"
    "v_mfma_f32_16x16x32_bf16 v[168:171], a[108:111], v[44:47], v[168:171]\n"
    "ds_write_addtid_b32 v165 offset:1472\n"
    "v_mfma_f32_16x16x32_bf16 v[168:171], a[112:115], v[48:51], v[168:171]\n"
    "ds_write_addtid_b32 v166 offset:1824\n"
    "v_mfma_f32_16x16x32_bf16 v[168:171], a[116:119], v[52:55], v[168:171]\n"
    "ds_write_addtid_b32 v167 offset:2112\n"
    "global_load_dwordx4 v[148:151], v250, s[14:15] offset:-1024 sc1\n"
    "v_mfma_f32_16x16x32_bf16 v[168:171], a[120:123], v[56:59], v[168:171]\n"
    "v_mfma_f32_16x16x32_bf16 v[168:171], a[124:127], v[60:63], v[168:171]\n"
    "s_waitcnt lgkmcnt(8)\n"
    "v_mfma_f32_16x16x32_bf16 v[172:175], a[96:99], v[64:67], v[172:175]\n"
    "s_waitcnt lgkmcnt(0)\n"
    "v_mfma_f32_16x16x32_bf16 v[172:175], a[100:103], v[68:71], v[172:175]\n"
    "s_barrier\n"
    "v_mfma_f32_16x16x32_bf16 v[172:175], a[104:107], v[72:75], v[172:175]\n"
    "ds_read_b128 v[160:163], v13 offset:0\n"
    "global_load_dwordx4 v[120:123], v250, s[14:15] offset:0 sc1\n"
    "v_mfma_f32_16x16x32_bf16 v[172:175], a[108:111], v[76:79], v[172:175]\n"
    "ds_read_b128 v[164:167], v13 offset:1184\n"
    "v_mfma_f32_16x16x32_bf16 v[172:175], a[112:115], v[80:83], v[172:175]\n"
    "ds_write_addtid_b32 v168 offset:2368\n"
    "v_mfma_f32_16x16x32_bf16 v[172:175], a[116:119], v[84:87], v[172:175]\n"
    "ds_write_addtid_b32 v169 offset:2656\n"
    "global_load_dwordx4 v[124:127], v250, s[14:15] offset:1024 sc1\n"
    "v_mfma_f32_16x16x32_bf16 v[172:175], a[120:123], v[88:91], v[172:175]\n"
    "ds_write_addtid_b32 v170 offset:3008\n"
    "v_mfma_f32_16x16x32_bf16 v[172:175], a[124:127], v[92:95], v[172:175]\n"
    "ds_write_addtid_b32 v171 offset:3296\n"
    "s_waitcnt lgkmcnt(5)\n"
    "v_mfma_f32_16x16x32_bf16 v[160:163], v[32:35], a[128:131], v[160:163]\n"
    "v_mfma_f32_16x16x32_bf16 v[160:163], v[36:39], a[132:135], v[160:163]\n"
    "global_load_dwordx4 v[152:155], v250, s[14:15] offset:2048 sc1\n"
    "v_mfma_f32_16x16x32_bf16 v[160:163], v[40:43], a[136:139], v[160:163]\n"
    "ds_write_addtid_b32 v172 offset:3552\n"
    "v_mfma_f32_16x16x32_bf16 v[160:163], v[44:47], a[140:143], v[160:163]\n"
    "ds_write_addtid_b32 v173 offset:3840\n"
    "v_mfma_f32_16x16x32_bf16 v[160:163], v[48:51], a[144:147], v[160:163]\n"
    "ds_write_addtid_b32 v174 offset:4192\n"
    "v_mfma_f32_16x16x32_bf16 v[160:163], v[52:55], a[148:151], v[160:163]\n"
    "ds_write_addtid_b32 v175 offset:4480\n"
    "global_load_dwordx4 v[156:159], v250, s[14:15] offset:3072 sc1\n"
    "v_mfma_f32_16x16x32_bf16 v[160:163], v[56:59], a[152:155], v[160:163]\n"
    "v_mfma_f32_16x16x32_bf16 v[160:163], v[60:63], a[156:159], v[160:163]\n"
    "s_waitcnt lgkmcnt(8)\n"
    "v_mfma_f32_16x16x32_bf16 v[164:167], v[64:67], a[128:131], v[164:167]\n"
    "global_store_dwordx2 v14, v[192:193], s[52:53] offset:128 sc0 sc1 nt\n"
    "s_waitcnt lgkmcnt(0)\n"
    "v_mfma_f32_16x16x32_bf16 v[164:167], v[68:71], a[132:135], v[164:167]\n"
    "s_barrier\n"
    "v_mfma_f32_16x16x32_bf16 v[164:167], v[72:75], a[136:139], v[164:167]\n"
    "ds_read_b128 v[168:171], v13 offset:2368\n"
    "v_mfma_f32_16x16x32_bf16 v[164:167], v[76:79], a[140:143], v[164:167]\n"
    "ds_read_b128 v[172:175], v13 offset:3552\n"
    "v_mfma_f32_16x16x32_bf16 v[164:167], v[80:83], a[144:147], v[164:167]\n"
    "ds_write_addtid_b32 v160 offset:0\n"
    "global_store_dwordx2 v14, v[194:195], s[54:55] offset:128 sc0 sc1 nt\n"
    "v_mfma_f32_16x16x32_bf16 v[164:167], v[84:87], a[148:151], v[164:167]\n"
    "ds_write_addtid_b32 v161 offset:288\n"
    "v_mfma_f32_16x16x32_bf16 v[164:167], v[88:91], a[152:155], v[164:167]\n"
    "ds_write_addtid_b32 v162 offset:640\n"
    "v_mfma_f32_16x16x32_bf16 v[164:167], v[92:95], a[156:159], v[164:167]\n"
    "ds_write_addtid_b32 v163 offset:928\n"
    "s_waitcnt lgkmcnt(5)\n"
    "v_mfma_f32_16x16x32_bf16 v[168:171], v[32:35], a[160:163], v[168:171]\n"
    "global_store_dwordx2 v14, v[196:197], s[56:57] offset:128 sc0 sc1 nt\n"
    "v_mfma_f32_16x16x32_bf16 v[168:171], v[36:39], a[164:167], v[168:171]\n"
    "v_mfma_f32_16x16x32_bf16 v[168:171], v[40:43], a[168:171], v[168:171]\n"
    "ds_write_addtid_b32 v164 offset:1184\n"
    "v_mfma_f32_16x16x32_bf16 v[168:171], v[44:47], a[172:175], v[168:171]\n"
    "ds_write_addtid_b32 v165 offset:1472\n"
    "v_mfma_f32_16x16x32_bf16 v[168:171], v[48:51], a[176:179], v[168:171]\n"
    "ds_write_addtid_b32 v166 offset:1824\n"
    "global_store_dwordx2 v14, v[198:199], s[58:59] offset:128 sc0 sc1 nt\n"
    "v_mfma_f32_16x16x32_bf16 v[168:171], v[52:55], a[180:183], v[168:171]\n"
    "ds_write_addtid_b32 v167 offset:2112\n"
    "v_mfma_f32_16x16x32_bf16 v[168:171], v[56:59], a[184:187], v[168:171]\n"
    "v_mfma_f32_16x16x32_bf16 v[168:171], v[60:63], a[188:191], v[168:171]\n"
    "s_waitcnt lgkmcnt(8)\n"
    "v_mfma_f32_16x16x32_bf16 v[172:175], v[64:67], a[160:163], v[172:175]\n"
    "global_store_dwordx2 v15, v[200:201], s[52:53] offset:128 sc0 sc1 nt\n"
    "s_waitcnt lgkmcnt(0)\n"
    "v_mfma_f32_16x16x32_bf16 v[172:175], v[68:71], a[164:167], v[172:175]\n"
    "s_barrier\n"
    "v_mfma_f32_16x16x32_bf16 v[172:175], v[72:75], a[168:171], v[172:175]\n"
    "ds_read_b128 v[160:163], v13 offset:0\n"
    "v_mfma_f32_16x16x32_bf16 v[172:175], v[76:79], a[172:175], v[172:175]\n"
    "ds_read_b128 v[164:167], v13 offset:1184\n"
    "v_mfma_f32_16x16x32_bf16 v[172:175], v[80:83], a[176:179], v[172:175]\n"
    "ds_write_addtid_b32 v168 offset:2368\n"
    "global_store_dwordx2 v15, v[202:203], s[54:55] offset:128 sc0 sc1 nt\n"
    "v_mfma_f32_16x16x32_bf16 v[172:175], v[84:87], a[180:183], v[172:175]\n"
    "ds_write_addtid_b32 v169 offset:2656\n"
    "v_mfma_f32_16x16x32_bf16 v[172:175], v[88:91], a[184:187], v[172:175]\n"
    "ds_write_addtid_b32 v170 offset:3008\n"
    "v_mfma_f32_16x16x32_bf16 v[172:175], v[92:95], a[188:191], v[172:175]\n"
    "ds_write_addtid_b32 v171 offset:3296\n"
    "s_waitcnt lgkmcnt(5)\n"
    "v_mfma_f32_16x16x32_bf16 v[160:163], a[192:195], v[32:35], v[160:163]\n"
    "global_store_dwordx2 v15, v[204:205], s[56:57] offset:128 sc0 sc1 nt\n"
    "v_mfma_f32_16x16x32_bf16 v[160:163], a[196:199], v[36:39], v[160:163]\n"
    "v_mfma_f32_16x16x32_bf16 v[160:163], a[200:203], v[40:43], v[160:163]\n"
    "ds_write_addtid_b32 v172 offset:3552\n"
    "v_mfma_f32_16x16x32_bf16 v[160:163], a[204:207], v[44:47], v[160:163]\n"
    "ds_write_addtid_b32 v173 offset:3840\n"
    "v_mfma_f32_16x16x32_bf16 v[160:163], a[208:211], v[48:51], v[160:163]\n"
    "ds_write_addtid_b32 v174 offset:4192\n"
    "global_store_dwordx2 v15, v[206:207], s[58:59] offset:128 sc0 sc1 nt\n"
    "v_mfma_f32_16x16x32_bf16 v[160:163], a[212:215], v[52:55], v[160:163]\n"
    "ds_write_addtid_b32 v175 offset:4480\n"
    "v_mfma_f32_16x16x32_bf16 v[160:163], a[216:219], v[56:59], v[160:163]\n"
    "v_mfma_f32_16x16x32_bf16 v[160:163], a[220:223], v[60:63], v[160:163]\n"
    "s_waitcnt lgkmcnt(8)\n"
    "v_mfma_f32_16x16x32_bf16 v[164:167], a[192:195], v[64:67], v[164:167]\n"
    "s_waitcnt lgkmcnt(0)\n"
    "v_mfma_f32_16x16x32_bf16 v[164:167], a[196:199], v[68:71], v[164:167]\n"
    "s_barrier\n"
    "v_mfma_f32_16x16x32_bf16 v[164:167], a[200:203], v[72:75], v[164:167]\n"
    "ds_read_b128 v[168:171], v13 offset:2368\n"
    "v_mfma_f32_16x16x32_bf16 v[164:167], a[204:207], v[76:79], v[164:167]\n"
    "ds_read_b128 v[172:175], v13 offset:3552\n"
    "v_mfma_f32_16x16x32_bf16 v[164:167], a[208:211], v[80:83], v[164:167]\n"
    "v_mfma_f32_16x16x32_bf16 v[164:167], a[212:215], v[84:87], v[164:167]\n"
    "v_mfma_f32_16x16x32_bf16 v[164:167], a[216:219], v[88:91], v[164:167]\n"
    "v_mfma_f32_16x16x32_bf16 v[164:167], a[220:223], v[92:95], v[164:167]\n"
    "s_waitcnt lgkmcnt(1)\n"
    "v_mfma_f32_16x16x32_bf16 v[168:171], a[224:227], v[32:35], v[168:171]\n"
    "v_mfma_f32_16x16x32_bf16 v[168:171], a[228:231], v[36:39], v[168:171]\n"
    "v_mfma_f32_16x16x32_bf16 v[168:171], a[232:235], v[40:43], v[168:171]\n"
    "v_cvt_pk_bf16_f32 v176, v160, v164\n"
    "v_cvt_pk_bf16_f32 v178, v161, v165\n"
    "v_mfma_f32_16x16x32_bf16 v[168:171], a[236:239], v[44:47], v[168:171]\n"
    "v_cvt_pk_bf16_f32 v180, v162, v166\n"
    "v_cvt_pk_bf16_f32 v182, v163, v167\n"
    "v_mfma_f32_16x16x32_bf16 v[168:171], a[240:243], v[48:51], v[168:171]\n"
    "v_mfma_f32_16x16x32_bf16 v[168:171], a[244:247], v[52:55], v[168:171]\n"
    "v_mfma_f32_16x16x32_bf16 v[168:171], a[248:251], v[56:59], v[168:171]\n"
    "v_mfma_f32_16x16x32_bf16 v[168:171], a[252:255], v[60:63], v[168:171]\n"
    "s_waitcnt lgkmcnt(0)\n"
    "v_mfma_f32_16x16x32_bf16 v[172:175], a[224:227], v[64:67], v[172:175]\n"
    "v_mfma_f32_16x16x32_bf16 v[172:175], a[228:231], v[68:71], v[172:175]\n"
    "v_mfma_f32_16x16x32_bf16 v[172:175], a[232:235], v[72:75], v[172:175]\n"
    "v_mfma_f32_16x16x32_bf16 v[172:175], a[236:239], v[76:79], v[172:175]\n"
    "v_mfma_f32_16x16x32_bf16 v[172:175], a[240:243], v[80:83], v[172:175]\n"
    "v_mfma_f32_16x16x32_bf16 v[172:175], a[244:247], v[84:87], v[172:175]\n"
    "v_mfma_f32_16x16x32_bf16 v[172:175], a[248:251], v[88:91], v[172:175]\n"
    "v_mfma_f32_16x16x32_bf16 v[172:175], a[252:255], v[92:95], v[172:175]\n"
    "s_waitcnt vmcnt(10)\n"
    "v_mfma_f32_16x16x32_bf16 v[160:163], v[96:99], a[0:3], 0\n"
    "v_mfma_f32_16x16x32_bf16 v[160:163], v[100:103], a[4:7], v[160:163]\n"
    "v_mfma_f32_16x16x32_bf16 v[160:163], v[104:107], a[8:11], v[160:163]\n"
    "v_cvt_pk_bf16_f32 v184, v168, v172\n"
    "v_cvt_pk_bf16_f32 v186, v169, v173\n"
    "v_mfma_f32_16x16x32_bf16 v[160:163], v[108:111], a[12:15], v[160:163]\n"
    "v_cvt_pk_bf16_f32 v188, v170, v174\n"
    "v_cvt_pk_bf16_f32 v190, v171, v175\n"
    "v_mfma_f32_16x16x32_bf16 v[160:163], v[112:115], a[16:19], v[160:163]\n"
    "v_mfma_f32_16x16x32_bf16 v[160:163], v[116:119], a[20:23], v[160:163]\n"
    "v_add_u32 v248, 131072, v248\n"
    "v_mfma_f32_16x16x32_bf16 v[160:163], v[120:123], a[24:27], v[160:163]\n"
    "v_mfma_f32_16x16x32_bf16 v[160:163], v[124:127], a[28:31], v[160:163]\n"
    "s_waitcnt vmcnt(8)\n"
    "v_mfma_f32_16x16x32_bf16 v[164:167], v[128:131], a[0:3], 0\n"
    "v_mfma_f32_16x16x32_bf16 v[164:167], v[132:135], a[4:7], v[164:167]\n"
    "global_load_dwordx4 v[32:35], v248, s[14:15] offset:-4096 sc1\n"
    "v_add_u32 v250, 8192, v248\n"
    "v_mfma_f32_16x16x32_bf16 v[164:167], v[136:139], a[8:11], v[164:167]\n"
    "v_mfma_f32_16x16x32_bf16 v[164:167], v[140:143], a[12:15], v[164:167]\n"
    "v_mfma_f32_16x16x32_bf16 v[164:167], v[144:147], a[16:19], v[164:167]\n"
    "ds_write_addtid_b32 v160 offset:0\n"
    "v_mfma_f32_16x16x32_bf16 v[164:167], v[148:151], a[20:23], v[164:167]\n"
    "ds_write_addtid_b32 v161 offset:288\n"
    "global_load_dwordx4 v[36:39], v248, s[14:15] offset:-3072 sc1\n"
    "v_mfma_f32_16x16x32_bf16 v[164:167], v[152:155], a[24:27], v[164:167]\n"
    "ds_write_addtid_b32 v162 offset:640\n"
    "v_mfma_f32_16x16x32_bf16 v[164:167], v[156:159], a[28:31], v[164:167]\n"
    "ds_write_addtid_b32 v163 offset:928\n"
    "v_mfma_f32_16x16x32_bf16 v[168:171], v[96:99], a[32:35], 0\n"
    "v_mfma_f32_16x16x32_bf16 v[168:171], v[100:103], a[36:39], v[168:171]\n"
    "global_load_dwordx4 v[64:67], v248, s[14:15] offset:-2048 sc1\n"
    "v_mfma_f32_16x16x32_bf16 v[168:171], v[104:107], a[40:43], v[168:171]\n"
    "ds_write_addtid_b32 v164 offset:1184\n"
    "v_mfma_f32_16x16x32_bf16 v[168:171], v[108:111], a[44:47], v[168:171]\n"
    "ds_write_addtid_b32 v165 offset:1472\n"
    "v_mfma_f32_16x16x32_bf16 v[168:171], v[112:115], a[48:51], v[168:171]\n"
    "ds_write_addtid_b32 v166 offset:1824\n"
    "v_mfma_f32_16x16x32_bf16 v[168:171], v[116:119], a[52:55], v[168:171]\n"
    "ds_write_addtid_b32 v167 offset:2112\n"
    "global_load_dwordx4 v[68:71], v248, s[14:15] offset:-1024 sc1\n"
    "v_mfma_f32_16x16x32_bf16 v[168:171], v[120:123], a[56:59], v[168:171]\n"
    "v_mfma_f32_16x16x32_bf16 v[168:171], v[124:127], a[60:63], v[168:171]\n"
    "v_mfma_f32_16x16x32_bf16 v[172:175], v[128:131], a[32:35], 0\n"
    "s_waitcnt lgkmcnt(0)\n"
    "v_mfma_f32_16x16x32_bf16 v[172:175], v[132:135], a[36:39], v[172:175]\n"
    "s_barrier\n"
    "v_mfma_f32_16x16x32_bf16 v[172:175], v[136:139], a[40:43], v[172:175]\n"
    "ds_read_b128 v[160:163], v13 offset:0\n"
    "global_load_dwordx4 v[40:43], v248, s[14:15] offset:0 sc1\n"
    "v_mfma_f32_16x16x32_bf16 v[172:175], v[140:143], a[44:47], v[172:175]\n"
    "ds_read_b128 v[164:167], v13 offset:1184\n"
    "v_mfma_f32_16x16x32_bf16 v[172:175], v[144:147], a[48:51], v[172:175]\n"
    "ds_write_addtid_b32 v168 offset:2368\n"
    "v_mfma_f32_16x16x32_bf16 v[172:175], v[148:151], a[52:55], v[172:175]\n"
    "ds_write_addtid_b32 v169 offset:2656\n"
    "global_load_dwordx4 v[44:47], v248, s[14:15] offset:1024 sc1\n"
    "v_mfma_f32_16x16x32_bf16 v[172:175], v[152:155], a[56:59], v[172:175]\n"
    "ds_write_addtid_b32 v170 offset:3008\n"
    "v_mfma_f32_16x16x32_bf16 v[172:175], v[156:159], a[60:63], v[172:175]\n"
    "ds_write_addtid_b32 v171 offset:3296\n"
    "s_waitcnt lgkmcnt(5)\n"
    "v_mfma_f32_16x16x32_bf16 v[160:163], a[64:67], v[96:99], v[160:163]\n"
    "v_mfma_f32_16x16x32_bf16 v[160:163], a[68:71], v[100:103], v[160:163]\n"
    "global_load_dwordx4 v[72:75], v248, s[14:15] offset:2048 sc1\n"
    "v_mfma_f32_16x16x32_bf16 v[160:163], a[72:75], v[104:107], v[160:163]\n"
    "ds_write_addtid_b32 v172 offset:3552\n"
    "v_mfma_f32_16x16x32_bf16 v[160:163], a[76:79], v[108:111], v[160:163]\n"
    "ds_write_addtid_b32 v173 offset:3840\n"
    "v_mfma_f32_16x16x32_bf16 v[160:163], a[80:83], v[112:115], v[160:163]\n"
    "ds_write_addtid_b32 v174 offset:4192\n"
    "v_mfma_f32_16x16x32_bf16 v[160:163], a[84:87], v[116:119], v[160:163]\n"
    "ds_write_addtid_b32 v175 offset:4480\n"
    "global_load_dwordx4 v[76:79], v248, s[14:15] offset:3072 sc1\n"
    "v_mfma_f32_16x16x32_bf16 v[160:163], a[88:91], v[120:123], v[160:163]\n"
    "v_mfma_f32_16x16x32_bf16 v[160:163], a[92:95], v[124:127], v[160:163]\n"
    "s_waitcnt lgkmcnt(8)\n"
    "v_mfma_f32_16x16x32_bf16 v[164:167], a[64:67], v[128:131], v[164:167]\n"
    "s_waitcnt lgkmcnt(0)\n"
    "v_mfma_f32_16x16x32_bf16 v[164:167], a[68:71], v[132:135], v[164:167]\n"
    "s_barrier\n"
    "v_mfma_f32_16x16x32_bf16 v[164:167], a[72:75], v[136:139], v[164:167]\n"
    "ds_read_b128 v[168:171], v13 offset:2368\n"
    "global_load_dwordx4 v[48:51], v250, s[14:15] offset:-4096 sc1\n"
    "v_mfma_f32_16x16x32_bf16 v[164:167], a[76:79], v[140:143], v[164:167]\n"
    "ds_read_b128 v[172:175], v13 offset:3552\n"
    "v_mfma_f32_16x16x32_bf16 v[164:167], a[80:83], v[144:147], v[164:167]\n"
    "ds_write_addtid_b32 v160 offset:0\n"
    "v_mfma_f32_16x16x32_bf16 v[164:167], a[84:87], v[148:151], v[164:167]\n"
    "ds_write_addtid_b32 v161 offset:288\n"
    "global_load_dwordx4 v[52:55], v250, s[14:15] offset:-3072 sc1\n"
    "v_mfma_f32_16x16x32_bf16 v[164:167], a[88:91], v[152:155], v[164:167]\n"
    "ds_write_addtid_b32 v162 offset:640\n"
    "v_mfma_f32_16x16x32_bf16 v[164:167], a[92:95], v[156:159], v[164:167]\n"
    "ds_write_addtid_b32 v163 offset:928\n"
    "s_waitcnt lgkmcnt(5)\n"
    "v_mfma_f32_16x16x32_bf16 v[168:171], a[96:99], v[96:99], v[168:171]\n"
    "v_mfma_f32_16x16x32_bf16 v[168:171], a[100:103], v[100:103], v[168:171]\n"
    "global_load_dwordx4 v[80:83], v250, s[14:15] offset:-2048 sc1\n"
    "v_mfma_f32_16x16x32_bf16 v[168:171], a[104:107], v[104:107], v[168:171]\n"
    "ds_write_addtid_b32 v164 offset:1184\n"
    "v_mfma_f32_16x16x32_bf16 v[168:171], a[108:111], v[108:111], v[168:171]\n"
    "ds_write_addtid_b32 v165 offset:1472\n"
    "v_mfma_f32_16x16x32_bf16 v[168:171], a[112:115], v[112:115], v[168:171]\n"
    "ds_write_addtid_b32 v166 offset:1824\n"
    "v_mfma_f32_16x16x32_bf16 v[168:171], a[116:119], v[116:119], v[168:171]\n"
    "ds_write_addtid_b32 v167 offset:2112\n"
    "global_load_dwordx4 v[84:87], v250, s[14:15] offset:-1024 sc1\n"
    "v_mfma_f32_16x16x32_bf16 v[168:171], a[120:123], v[120:123], v[168:171]\n"
    "v_mfma_f32_16x16x32_bf16 v[168:171], a[124:127], v[124:127], v[168:171]\n"
    "s_waitcnt lgkmcnt(8)\n"
    "v_mfma_f32_16x16x32_bf16 v[172:175], a[96:99], v[128:131], v[172:175]\n"
    "s_waitcnt lgkmcnt(0)\n"
    "v_mfma_f32_16x16x32_bf16 v[172:175], a[100:103], v[132:135], v[172:175]\n"
    "s_barrier\n"
    "v_mfma_f32_16x16x32_bf16 v[172:175], a[104:107], v[136:139], v[172:175]\n"
    "ds_read_b128 v[160:163], v13 offset:0\n"
    "global_load_dwordx4 v[56:59], v250, s[14:15] offset:0 sc1\n"
    "v_mfma_f32_16x16x32_bf16 v[172:175], a[108:111], v[140:143], v[172:175]\n"
    "ds_read_b128 v[164:167], v13 offset:1184\n"
    "v_mfma_f32_16x16x32_bf16 v[172:175], a[112:115], v[144:147], v[172:175]\n"
    "ds_write_addtid_b32 v168 offset:2368\n"
    "v_mfma_f32_16x16x32_bf16 v[172:175], a[116:119], v[148:151], v[172:175]\n"
    "ds_write_addtid_b32 v169 offset:2656\n"
    "global_load_dwordx4 v[60:63], v250, s[14:15] offset:1024 sc1\n"
    "v_mfma_f32_16x16x32_bf16 v[172:175], a[120:123], v[152:155], v[172:175]\n"
    "ds_write_addtid_b32 v170 offset:3008\n"
    "v_mfma_f32_16x16x32_bf16 v[172:175], a[124:127], v[156:159], v[172:175]\n"
    "ds_write_addtid_b32 v171 offset:3296\n"
    "s_waitcnt lgkmcnt(5)\n"
    "v_mfma_f32_16x16x32_bf16 v[160:163], v[96:99], a[128:131], v[160:163]\n"
    "v_mfma_f32_16x16x32_bf16 v[160:163], v[100:103], a[132:135], v[160:163]\n"
    "global_load_dwordx4 v[88:91], v250, s[14:15] offset:2048 sc1\n"
    "v_mfma_f32_16x16x32_bf16 v[160:163], v[104:107], a[136:139], v[160:163]\n"
    "ds_write_addtid_b32 v172 offset:3552\n"
    "v_mfma_f32_16x16x32_bf16 v[160:163], v[108:111], a[140:143], v[160:163]\n"
    "ds_write_addtid_b32 v173 offset:3840\n"
    "v_mfma_f32_16x16x32_bf16 v[160:163], v[112:115], a[144:147], v[160:163]\n"
    "ds_write_addtid_b32 v174 offset:4192\n"
    "v_mfma_f32_16x16x32_bf16 v[160:163], v[116:119], a[148:151], v[160:163]\n"
    "ds_write_addtid_b32 v175 offset:4480\n"
    "global_load_dwordx4 v[92:95], v250, s[14:15] offset:3072 sc1\n"
    "v_mfma_f32_16x16x32_bf16 v[160:163], v[120:123], a[152:155], v[160:163]\n"
    "v_mfma_f32_16x16x32_bf16 v[160:163], v[124:127], a[156:159], v[160:163]\n"
    "s_waitcnt lgkmcnt(8)\n"
    "v_mfma_f32_16x16x32_bf16 v[164:167], v[128:131], a[128:131], v[164:167]\n"
    "s_waitcnt lgkmcnt(0)\n"
    "v_mfma_f32_16x16x32_bf16 v[164:167], v[132:135], a[132:135], v[164:167]\n"
    "s_barrier\n"
    "v_mfma_f32_16x16x32_bf16 v[164:167], v[136:139], a[136:139], v[164:167]\n"
    "ds_read_b128 v[168:171], v13 offset:2368\n"
    "v_mfma_f32_16x16x32_bf16 v[164:167], v[140:143], a[140:143], v[164:167]\n"
    "ds_read_b128 v[172:175], v13 offset:3552\n"
    "v_mfma_f32_16x16x32_bf16 v[164:167], v[144:147], a[144:147], v[164:167]\n"
    "ds_write_addtid_b32 v160 offset:0\n"
    "v_mfma_f32_16x16x32_bf16 v[164:167], v[148:151], a[148:151], v[164:167]\n"
    "ds_write_addtid_b32 v161 offset:288\n"
    "v_mfma_f32_16x16x32_bf16 v[164:167], v[152:155], a[152:155], v[164:167]\n"
    "ds_write_addtid_b32 v162 offset:640\n"
    "v_mfma_f32_16x16x32_bf16 v[164:167], v[156:159], a[156:159], v[164:167]\n"
    "ds_write_addtid_b32 v163 offset:928\n"
    "s_waitcnt lgkmcnt(5)\n"
    "v_mfma_f32_16x16x32_bf16 v[168:171], v[96:99], a[160:163], v[168:171]\n"
    "v_mfma_f32_16x16x32_bf16 v[168:171], v[100:103], a[164:167], v[168:171]\n"
    "v_mfma_f32_16x16x32_bf16 v[168:171], v[104:107], a[168:171], v[168:171]\n"
    "ds_write_addtid_b32 v164 offset:1184\n"
    "v_mfma_f32_16x16x32_bf16 v[168:171], v[108:111], a[172:175], v[168:171]\n"
    "ds_write_addtid_b32 v165 offset:1472\n"
    "v_mfma_f32_16x16x32_bf16 v[168:171], v[112:115], a[176:179], v[168:171]\n"
    "ds_write_addtid_b32 v166 offset:1824\n"
    "v_mfma_f32_16x16x32_bf16 v[168:171], v[116:119], a[180:183], v[168:171]\n"
    "ds_write_addtid_b32 v167 offset:2112\n"
    "v_mfma_f32_16x16x32_bf16 v[168:171], v[120:123], a[184:187], v[168:171]\n"
    "v_mfma_f32_16x16x32_bf16 v[168:171], v[124:127], a[188:191], v[168:171]\n"
    "s_waitcnt lgkmcnt(8)\n"
    "v_mfma_f32_16x16x32_bf16 v[172:175], v[128:131], a[160:163], v[172:175]\n"
    "s_waitcnt lgkmcnt(0)\n"
    "v_mfma_f32_16x16x32_bf16 v[172:175], v[132:135], a[164:167], v[172:175]\n"
    "s_barrier\n"
    "v_mfma_f32_16x16x32_bf16 v[172:175], v[136:139], a[168:171], v[172:175]\n"
    "ds_read_b128 v[160:163], v13 offset:0\n"
    "v_mfma_f32_16x16x32_bf16 v[172:175], v[140:143], a[172:175], v[172:175]\n"
    "ds_read_b128 v[164:167], v13 offset:1184\n"
    "v_mfma_f32_16x16x32_bf16 v[172:175], v[144:147], a[176:179], v[172:175]\n"
    "ds_write_addtid_b32 v168 offset:2368\n"
    "v_mfma_f32_16x16x32_bf16 v[172:175], v[148:151], a[180:183], v[172:175]\n"
    "ds_write_addtid_b32 v169 offset:2656\n"
    "v_mfma_f32_16x16x32_bf16 v[172:175], v[152:155], a[184:187], v[172:175]\n"
    "ds_write_addtid_b32 v170 offset:3008\n"
    "v_mfma_f32_16x16x32_bf16 v[172:175], v[156:159], a[188:191], v[172:175]\n"
    "ds_write_addtid_b32 v171 offset:3296\n"
    "s_waitcnt lgkmcnt(5)\n"
    "v_mfma_f32_16x16x32_bf16 v[160:163], a[192:195], v[96:99], v[160:163]\n"
    "v_mfma_f32_16x16x32_bf16 v[160:163], a[196:199], v[100:103], v[160:163]\n"
    "v_mfma_f32_16x16x32_bf16 v[160:163], a[200:203], v[104:107], v[160:163]\n"
    "ds_write_addtid_b32 v172 offset:3552\n"
    "v_mfma_f32_16x16x32_bf16 v[160:163], a[204:207], v[108:111], v[160:163]\n"
    "ds_write_addtid_b32 v173 offset:3840\n"
    "v_mfma_f32_16x16x32_bf16 v[160:163], a[208:211], v[112:115], v[160:163]\n"
    "ds_write_addtid_b32 v174 offset:4192\n"
    "v_mfma_f32_16x16x32_bf16 v[160:163], a[212:215], v[116:119], v[160:163]\n"
    "ds_write_addtid_b32 v175 offset:4480\n"
    "v_mfma_f32_16x16x32_bf16 v[160:163], a[216:219], v[120:123], v[160:163]\n"
    "v_mfma_f32_16x16x32_bf16 v[160:163], a[220:223], v[124:127], v[160:163]\n"
    "s_waitcnt lgkmcnt(8)\n"
    "v_mfma_f32_16x16x32_bf16 v[164:167], a[192:195], v[128:131], v[164:167]\n"
    "s_waitcnt lgkmcnt(0)\n"
    "v_mfma_f32_16x16x32_bf16 v[164:167], a[196:199], v[132:135], v[164:167]\n"
    "s_barrier\n"
    "v_mfma_f32_16x16x32_bf16 v[164:167], a[200:203], v[136:139], v[164:167]\n"
    "ds_read_b128 v[168:171], v13 offset:2368\n"
    "v_mfma_f32_16x16x32_bf16 v[164:167], a[204:207], v[140:143], v[164:167]\n"
    "ds_read_b128 v[172:175], v13 offset:3552\n"
    "v_mfma_f32_16x16x32_bf16 v[164:167], a[208:211], v[144:147], v[164:167]\n"
    "v_mfma_f32_16x16x32_bf16 v[164:167], a[212:215], v[148:151], v[164:167]\n"
    "v_mfma_f32_16x16x32_bf16 v[164:167], a[216:219], v[152:155], v[164:167]\n"
    "v_mfma_f32_16x16x32_bf16 v[164:167], a[220:223], v[156:159], v[164:167]\n"
    "s_waitcnt lgkmcnt(1)\n"
    "v_mfma_f32_16x16x32_bf16 v[168:171], a[224:227], v[96:99], v[168:171]\n"
    "v_mfma_f32_16x16x32_bf16 v[168:171], a[228:231], v[100:103], v[168:171]\n"
    "v_mfma_f32_16x16x32_bf16 v[168:171], a[232:235], v[104:107], v[168:171]\n"
    "v_cvt_pk_bf16_f32 v177, v160, v164\n"
    "v_cvt_pk_bf16_f32 v179, v161, v165\n"
    "v_mfma_f32_16x16x32_bf16 v[168:171], a[236:239], v[108:111], v[168:171]\n"
    "v_cvt_pk_bf16_f32 v181, v162, v166\n"
    "v_cvt_pk_bf16_f32 v183, v163, v167\n"
    "v_mfma_f32_16x16x32_bf16 v[168:171], a[240:243], v[112:115], v[168:171]\n"
    "v_mfma_f32_16x16x32_bf16 v[168:171], a[244:247], v[116:119], v[168:171]\n"
    "v_mfma_f32_16x16x32_bf16 v[168:171], a[248:251], v[120:123], v[168:171]\n"
    "v_mfma_f32_16x16x32_bf16 v[168:171], a[252:255], v[124:127], v[168:171]\n"
    "s_waitcnt lgkmcnt(0)\n"
    "v_mfma_f32_16x16x32_bf16 v[172:175], a[224:227], v[128:131], v[172:175]\n"
    "v_mfma_f32_16x16x32_bf16 v[172:175], a[228:231], v[132:135], v[172:175]\n"
    "v_mfma_f32_16x16x32_bf16 v[172:175], a[232:235], v[136:139], v[172:175]\n"
    "v_mfma_f32_16x16x32_bf16 v[172:175], a[236:239], v[140:143], v[172:175]\n"
    "v_mfma_f32_16x16x32_bf16 v[172:175], a[240:243], v[144:147], v[172:175]\n"
    "v_mfma_f32_16x16x32_bf16 v[172:175], a[244:247], v[148:151], v[172:175]\n"
    "v_mfma_f32_16x16x32_bf16 v[172:175], a[248:251], v[152:155], v[172:175]\n"
    "v_mfma_f32_16x16x32_bf16 v[172:175], a[252:255], v[156:159], v[172:175]\n"
    "s_waitcnt vmcnt(2)\n"
    "v_mfma_f32_16x16x32_bf16 v[160:163], v[32:35], a[0:3], 0\n"
    "v_mfma_f32_16x16x32_bf16 v[160:163], v[36:39], a[4:7], v[160:163]\n"
    "v_mfma_f32_16x16x32_bf16 v[160:163], v[40:43], a[8:11], v[160:163]\n"
    "v_cvt_pk_bf16_f32 v185, v168, v172\n"
    "v_cvt_pk_bf16_f32 v187, v169, v173\n"
    "v_mfma_f32_16x16x32_bf16 v[160:163], v[44:47], a[12:15], v[160:163]\n"
    "v_cvt_pk_bf16_f32 v189, v170, v174\n"
    "v_cvt_pk_bf16_f32 v191, v171, v175\n"
    "v_mfma_f32_16x16x32_bf16 v[160:163], v[48:51], a[16:19], v[160:163]\n"
    "v_mfma_f32_16x16x32_bf16 v[160:163], v[52:55], a[20:23], v[160:163]\n"
    "v_mfma_f32_16x16x32_bf16 v[160:163], v[56:59], a[24:27], v[160:163]\n"
    "v_mfma_f32_16x16x32_bf16 v[160:163], v[60:63], a[28:31], v[160:163]\n"
    "s_waitcnt vmcnt(0)\n"
    "v_mfma_f32_16x16x32_bf16 v[164:167], v[64:67], a[0:3], 0\n"
    "global_store_dwordx2 v14, v[176:177], s[52:53] offset:256 sc0 sc1 nt\n"
    "v_mfma_f32_16x16x32_bf16 v[164:167], v[68:71], a[4:7], v[164:167]\n"
    "v_mfma_f32_16x16x32_bf16 v[164:167], v[72:75], a[8:11], v[164:167]\n"
    "v_mfma_f32_16x16x32_bf16 v[164:167], v[76:79], a[12:15], v[164:167]\n"
    "v_mfma_f32_16x16x32_bf16 v[164:167], v[80:83], a[16:19], v[164:167]\n"
    "ds_write_addtid_b32 v160 offset:0\n"
    "global_store_dwordx2 v14, v[178:179], s[54:55] offset:256 sc0 sc1 nt\n"
    "v_mfma_f32_16x16x32_bf16 v[164:167], v[84:87], a[20:23], v[164:167]\n"
    "ds_write_addtid_b32 v161 offset:288\n"
    "v_mfma_f32_16x16x32_bf16 v[164:167], v[88:91], a[24:27], v[164:167]\n"
    "ds_write_addtid_b32 v162 offset:640\n"
    "v_mfma_f32_16x16x32_bf16 v[164:167], v[92:95], a[28:31], v[164:167]\n"
    "ds_write_addtid_b32 v163 offset:928\n"
    "v_mfma_f32_16x16x32_bf16 v[168:171], v[32:35], a[32:35], 0\n"
    "global_store_dwordx2 v14, v[180:181], s[56:57] offset:256 sc0 sc1 nt\n"
    "v_mfma_f32_16x16x32_bf16 v[168:171], v[36:39], a[36:39], v[168:171]\n"
    "v_mfma_f32_16x16x32_bf16 v[168:171], v[40:43], a[40:43], v[168:171]\n"
    "ds_write_addtid_b32 v164 offset:1184\n"
    "v_mfma_f32_16x16x32_bf16 v[168:171], v[44:47], a[44:47], v[168:171]\n"
    "ds_write_addtid_b32 v165 offset:1472\n"
    "v_mfma_f32_16x16x32_bf16 v[168:171], v[48:51], a[48:51], v[168:171]\n"
    "ds_write_addtid_b32 v166 offset:1824\n"
    "global_store_dwordx2 v14, v[182:183], s[58:59] offset:256 sc0 sc1 nt\n"
    "v_mfma_f32_16x16x32_bf16 v[168:171], v[52:55], a[52:55], v[168:171]\n"
    "ds_write_addtid_b32 v167 offset:2112\n"
    "v_mfma_f32_16x16x32_bf16 v[168:171], v[56:59], a[56:59], v[168:171]\n"
    "v_mfma_f32_16x16x32_bf16 v[168:171], v[60:63], a[60:63], v[168:171]\n"
    "v_mfma_f32_16x16x32_bf16 v[172:175], v[64:67], a[32:35], 0\n"
    "global_store_dwordx2 v15, v[184:185], s[52:53] offset:256 sc0 sc1 nt\n"
    "s_waitcnt lgkmcnt(0)\n"
    "v_mfma_f32_16x16x32_bf16 v[172:175], v[68:71], a[36:39], v[172:175]\n"
    "s_barrier\n"
    "v_mfma_f32_16x16x32_bf16 v[172:175], v[72:75], a[40:43], v[172:175]\n"
    "ds_read_b128 v[160:163], v13 offset:0\n"
    "v_mfma_f32_16x16x32_bf16 v[172:175], v[76:79], a[44:47], v[172:175]\n"
    "ds_read_b128 v[164:167], v13 offset:1184\n"
    "v_mfma_f32_16x16x32_bf16 v[172:175], v[80:83], a[48:51], v[172:175]\n"
    "ds_write_addtid_b32 v168 offset:2368\n"
    "global_store_dwordx2 v15, v[186:187], s[54:55] offset:256 sc0 sc1 nt\n"
    "v_mfma_f32_16x16x32_bf16 v[172:175], v[84:87], a[52:55], v[172:175]\n"
    "ds_write_addtid_b32 v169 offset:2656\n"
    "v_mfma_f32_16x16x32_bf16 v[172:175], v[88:91], a[56:59], v[172:175]\n"
    "ds_write_addtid_b32 v170 offset:3008\n"
    "v_mfma_f32_16x16x32_bf16 v[172:175], v[92:95], a[60:63], v[172:175]\n"
    "ds_write_addtid_b32 v171 offset:3296\n"
    "s_waitcnt lgkmcnt(5)\n"
    "v_mfma_f32_16x16x32_bf16 v[160:163], a[64:67], v[32:35], v[160:163]\n"
    "global_store_dwordx2 v15, v[188:189], s[56:57] offset:256 sc0 sc1 nt\n"
    "v_mfma_f32_16x16x32_bf16 v[160:163], a[68:71], v[36:39], v[160:163]\n"
    "v_mfma_f32_16x16x32_bf16 v[160:163], a[72:75], v[40:43], v[160:163]\n"
    "ds_write_addtid_b32 v172 offset:3552\n"
    "v_mfma_f32_16x16x32_bf16 v[160:163], a[76:79], v[44:47], v[160:163]\n"
    "ds_write_addtid_b32 v173 offset:3840\n"
    "v_mfma_f32_16x16x32_bf16 v[160:163], a[80:83], v[48:51], v[160:163]\n"
    "ds_write_addtid_b32 v174 offset:4192\n"
    "global_store_dwordx2 v15, v[190:191], s[58:59] offset:256 sc0 sc1 nt\n"
    "v_mfma_f32_16x16x32_bf16 v[160:163], a[84:87], v[52:55], v[160:163]\n"
    "ds_write_addtid_b32 v175 offset:4480\n"
    "v_mfma_f32_16x16x32_bf16 v[160:163], a[88:91], v[56:59], v[160:163]\n"
    "HOP3_%=:\n"
    "s_cbranch_vccnz HOPDONE_%=\n"
    "v_mfma_f32_16x16x32_bf16 v[160:163], a[92:95], v[60:63], v[160:163]\n"
    "s_waitcnt lgkmcnt(8)\n"
    "v_mfma_f32_16x16x32_bf16 v[164:167], a[64:67], v[64:67], v[164:167]\n"
    "s_waitcnt lgkmcnt(0)\n"
    "v_mfma_f32_16x16x32_bf16 v[164:167], a[68:71], v[68:71], v[164:167]\n"
    "s_barrier\n"
    "v_mfma_f32_16x16x32_bf16 v[164:167], a[72:75], v[72:75], v[164:167]\n"
    "ds_read_b128 v[168:171], v13 offset:2368\n"
    "v_mfma_f32_16x16x32_bf16 v[164:167], a[76:79], v[76:79], v[164:167]\n"
    "ds_read_b128 v[172:175], v13 offset:3552\n"
    "v_mfma_f32_16x16x32_bf16 v[164:167], a[80:83], v[80:83], v[164:167]\n"
    "ds_write_addtid_b32 v160 offset:0\n"
    "v_mfma_f32_16x16x32_bf16 v[164:167], a[84:87], v[84:87], v[164:167]\n"
    "ds_write_addtid_b32 v161 offset:288\n"
    "v_mfma_f32_16x16x32_bf16 v[164:167], a[88:91], v[88:91], v[164:167]\n"
    "ds_write_addtid_b32 v162 offset:640\n"
    "v_mfma_f32_16x16x32_bf16 v[164:167], a[92:95], v[92:95], v[164:167]\n"
    "ds_write_addtid_b32 v163 offset:928\n"
    "s_waitcnt lgkmcnt(5)\n"
    "v_mfma_f32_16x16x32_bf16 v[168:171], a[96:99], v[32:35], v[168:171]\n"
    "v_mfma_f32_16x16x32_bf16 v[168:171], a[100:103], v[36:39], v[168:171]\n"
    "v_mfma_f32_16x16x32_bf16 v[168:171], a[104:107], v[40:43], v[168:171]\n"
    "ds_write_addtid_b32 v164 offset:1184\n"
    "v_mfma_f32_16x16x32_bf16 v[168:171], a[108:111], v[44:47], v[168:171]\n"
    "ds_write_addtid_b32 v165 offset:1472\n"
    "v_mfma_f32_16x16x32_bf16 v[168:171], a[112:115], v[48:51], v[168:171]\n"
    "ds_write_addtid_b32 v166 offset:1824\n"
    "v_mfma_f32_16x16x32_bf16 v[168:171], a[116:119], v[52:55], v[168:171]\n"
    "ds_write_addtid_b32 v167 offset:2112\n"
    "v_mfma_f32_16x16x32_bf16 v[168:171], a[120:123], v[56:59], v[168:171]\n"
    "v_mfma_f32_16x16x32_bf16 v[168:171], a[124:127], v[60:63], v[168:171]\n"
    "s_waitcnt lgkmcnt(8)\n"
    "v_mfma_f32_16x16x32_bf16 v[172:175], a[96:99], v[64:67], v[172:175]\n"
    "s_waitcnt lgkmcnt(0)\n"
    "v_mfma_f32_16x16x32_bf16 v[172:175], a[100:103], v[68:71], v[172:175]\n"
    "s_barrier\n"
    "v_mfma_f32_16x16x32_bf16 v[172:175], a[104:107], v[72:75], v[172:175]\n"
    "ds_read_b128 v[160:163], v13 offset:0\n"
    "v_mfma_f32_16x16x32_bf16 v[172:175], a[108:111], v[76:79], v[172:175]\n"
    "ds_read_b128 v[164:167], v13 offset:1184\n"
    "v_mfma_f32_16x16x32_bf16 v[172:175], a[112:115], v[80:83], v[172:175]\n"
    "ds_write_addtid_b32 v168 offset:2368\n"
    "v_mfma_f32_16x16x32_bf16 v[172:175], a[116:119], v[84:87], v[172:175]\n"
    "ds_write_addtid_b32 v169 offset:2656\n"
    "v_mfma_f32_16x16x32_bf16 v[172:175], a[120:123], v[88:91], v[172:175]\n"
    "ds_write_addtid_b32 v170 offset:3008\n"
    "v_mfma_f32_16x16x32_bf16 v[172:175], a[124:127], v[92:95], v[172:175]\n"
    "ds_write_addtid_b32 v171 offset:3296\n"
    "s_waitcnt lgkmcnt(5)\n"
    "v_mfma_f32_16x16x32_bf16 v[160:163], v[32:35], a[128:131], v[160:163]\n"
    "v_mfma_f32_16x16x32_bf16 v[160:163], v[36:39], a[132:135], v[160:163]\n"
    "v_mfma_f32_16x16x32_bf16 v[160:163], v[40:43], a[136:139], v[160:163]\n"
    "ds_write_addtid_b32 v172 offset:3552\n"
    "v_mfma_f32_16x16x32_bf16 v[160:163], v[44:47], a[140:143], v[160:163]\n"
    "ds_write_addtid_b32 v173 offset:3840\n"
    "v_mfma_f32_16x16x32_bf16 v[160:163], v[48:51], a[144:147], v[160:163]\n"
    "ds_write_addtid_b32 v174 offset:4192\n"
    "v_mfma_f32_16x16x32_bf16 v[160:163], v[52:55], a[148:151], v[160:163]\n"
    "ds_write_addtid_b32 v175 offset:4480\n"
    "v_mfma_f32_16x16x32_bf16 v[160:163], v[56:59], a[152:155], v[160:163]\n"
    "v_mfma_f32_16x16x32_bf16 v[160:163], v[60:63], a[156:159], v[160:163]\n"
    "s_waitcnt lgkmcnt(8)\n"
    "v_mfma_f32_16x16x32_bf16 v[164:167], v[64:67], a[128:131], v[164:167]\n"
    "s_waitcnt lgkmcnt(0)\n"
    "v_mfma_f32_16x16x32_bf16 v[164:167], v[68:71], a[132:135], v[164:167]\n"
    "s_barrier\n"
    "v_mfma_f32_16x16x32_bf16 v[164:167], v[72:75], a[136:139], v[164:167]\n"
    "ds_read_b128 v[168:171], v13 offset:2368\n"
    "v_mfma_f32_16x16x32_bf16 v[164:167], v[76:79], a[140:143], v[164:167]\n"
    "ds_read_b128 v[172:175], v13 offset:3552\n"
    "v_mfma_f32_16x16x32_bf16 v[164:167], v[80:83], a[144:147], v[164:167]\n"
    "ds_write_addtid_b32 v160 offset:0\n"
    "v_mfma_f32_16x16x32_bf16 v[164:167], v[84:87], a[148:151], v[164:167]\n"
    "ds_write_addtid_b32 v161 offset:288\n"
    "v_mfma_f32_16x16x32_bf16 v[164:167], v[88:91], a[152:155], v[164:167]\n"
    "ds_write_addtid_b32 v162 offset:640\n"
    "v_mfma_f32_16x16x32_bf16 v[164:167], v[92:95], a[156:159], v[164:167]\n"
    "ds_write_addtid_b32 v163 offset:928\n"
    "s_waitcnt lgkmcnt(5)\n"
    "v_mfma_f32_16x16x32_bf16 v[168:171], v[32:35], a[160:163], v[168:171]\n"
    "v_mfma_f32_16x16x32_bf16 v[168:171], v[36:39], a[164:167], v[168:171]\n"
    "v_mfma_f32_16x16x32_bf16 v[168:171], v[40:43], a[168:171], v[168:171]\n"
    "ds_write_addtid_b32 v164 offset:1184\n"
    "v_mfma_f32_16x16x32_bf16 v[168:171], v[44:47], a[172:175], v[168:171]\n"
    "ds_write_addtid_b32 v165 offset:1472\n"
    "v_mfma_f32_16x16x32_bf16 v[168:171], v[48:51], a[176:179], v[168:171]\n"
    "ds_write_addtid_b32 v166 offset:1824\n"
    "v_mfma_f32_16x16x32_bf16 v[168:171], v[52:55], a[180:183], v[168:171]\n"
    "ds_write_addtid_b32 v167 offset:2112\n"
    "v_mfma_f32_16x16x32_bf16 v[168:171], v[56:59], a[184:187], v[168:171]\n"
    "v_mfma_f32_16x16x32_bf16 v[168:171], v[60:63], a[188:191], v[168:171]\n"
    "s_waitcnt lgkmcnt(8)\n"
    "v_mfma_f32_16x16x32_bf16 v[172:175], v[64:67], a[160:163], v[172:175]\n"
    "s_waitcnt lgkmcnt(0)\n"
    "v_mfma_f32_16x16x32_bf16 v[172:175], v[68:71], a[164:167], v[172:175]\n"
    "s_barrier\n"
    "v_mfma_f32_16x16x32_bf16 v[172:175], v[72:75], a[168:171], v[172:175]\n"
    "ds_read_b128 v[160:163], v13 offset:0\n"
    "v_mfma_f32_16x16x32_bf16 v[172:175], v[76:79], a[172:175], v[172:175]\n"
    "ds_read_b128 v[164:167], v13 offset:1184\n"
    "v_mfma_f32_16x16x32_bf16 v[172:175], v[80:83], a[176:179], v[172:175]\n"
    "ds_write_addtid_b32 v168 offset:2368\n"
    "v_mfma_f32_16x16x32_bf16 v[172:175], v[84:87], a[180:183], v[172:175]\n"
    "ds_write_addtid_b32 v169 offset:2656\n"
    "v_mfma_f32_16x16x32_bf16 v[172:175], v[88:91], a[184:187], v[172:175]\n"
    "ds_write_addtid_b32 v170 offset:3008\n"
    "v_mfma_f32_16x16x32_bf16 v[172:175], v[92:95], a[188:191], v[172:175]\n"
    "ds_write_addtid_b32 v171 offset:3296\n"
    "s_waitcnt lgkmcnt(5)\n"
    "v_mfma_f32_16x16x32_bf16 v[160:163], a[192:195], v[32:35], v[160:163]\n"
    "v_mfma_f32_16x16x32_bf16 v[160:163], a[196:199], v[36:39], v[160:163]\n"
    "v_mfma_f32_16x16x32_bf16 v[160:163], a[200:203], v[40:43], v[160:163]\n"
    "ds_write_addtid_b32 v172 offset:3552\n"
    "v_mfma_f32_16x16x32_bf16 v[160:163], a[204:207], v[44:47], v[160:163]\n"
    "ds_write_addtid_b32 v173 offset:3840\n"
    "v_mfma_f32_16x16x32_bf16 v[160:163], a[208:211], v[48:51], v[160:163]\n"
    "ds_write_addtid_b32 v174 offset:4192\n"
    "v_mfma_f32_16x16x32_bf16 v[160:163], a[212:215], v[52:55], v[160:163]\n"
    "ds_write_addtid_b32 v175 offset:4480\n"
    "v_mfma_f32_16x16x32_bf16 v[160:163], a[216:219], v[56:59], v[160:163]\n"
    "v_mfma_f32_16x16x32_bf16 v[160:163], a[220:223], v[60:63], v[160:163]\n"
    "s_waitcnt lgkmcnt(8)\n"
    "v_mfma_f32_16x16x32_bf16 v[164:167], a[192:195], v[64:67], v[164:167]\n"
    "s_waitcnt lgkmcnt(0)\n"
    "v_mfma_f32_16x16x32_bf16 v[164:167], a[196:199], v[68:71], v[164:167]\n"
    "s_barrier\n"
    "v_mfma_f32_16x16x32_bf16 v[164:167], a[200:203], v[72:75], v[164:167]\n"
    "ds_read_b128 v[168:171], v13 offset:2368\n"
    "v_mfma_f32_16x16x32_bf16 v[164:167], a[204:207], v[76:79], v[164:167]\n"
    "ds_read_b128 v[172:175], v13 offset:3552\n"
    "v_mfma_f32_16x16x32_bf16 v[164:167], a[208:211], v[80:83], v[164:167]\n"
    "v_mfma_f32_16x16x32_bf16 v[164:167], a[212:215], v[84:87], v[164:167]\n"
    "v_mfma_f32_16x16x32_bf16 v[164:167], a[216:219], v[88:91], v[164:167]\n"
    "v_mfma_f32_16x16x32_bf16 v[164:167], a[220:223], v[92:95], v[164:167]\n"
    "s_waitcnt lgkmcnt(1)\n"
    "v_mfma_f32_16x16x32_bf16 v[168:171], a[224:227], v[32:35], v[168:171]\n"
    "v_mfma_f32_16x16x32_bf16 v[168:171], a[228:231], v[36:39], v[168:171]\n"
    "v_mfma_f32_16x16x32_bf16 v[168:171], a[232:235], v[40:43], v[168:171]\n"
    "v_cvt_pk_bf16_f32 v192, v160, v164\n"
    "v_cvt_pk_bf16_f32 v194, v161, v165\n"
    "v_mfma_f32_16x16x32_bf16 v[168:171], a[236:239], v[44:47], v[168:171]\n"
    "v_cvt_pk_bf16_f32 v196, v162, v166\n"
    "v_cvt_pk_bf16_f32 v198, v163, v167\n"
    "v_mfma_f32_16x16x32_bf16 v[168:171], a[240:243], v[48:51], v[168:171]\n"
    "global_store_dword v16, v192, s[52:53] sc0 sc1 nt\n"
    "v_mfma_f32_16x16x32_bf16 v[168:171], a[244:247], v[52:55], v[168:171]\n"
    "global_store_dword v16, v194, s[54:55] sc0 sc1 nt\n"
    "v_mfma_f32_16x16x32_bf16 v[168:171], a[248:251], v[56:59], v[168:171]\n"
    "global_store_dword v16, v196, s[56:57] sc0 sc1 nt\n"
    "v_mfma_f32_16x16x32_bf16 v[168:171], a[252:255], v[60:63], v[168:171]\n"
    "global_store_dword v16, v198, s[58:59] sc0 sc1 nt\n"
    "s_waitcnt lgkmcnt(0)\n"
    "v_mfma_f32_16x16x32_bf16 v[172:175], a[224:227], v[64:67], v[172:175]\n"
    "v_mfma_f32_16x16x32_bf16 v[172:175], a[228:231], v[68:71], v[172:175]\n"
    "v_mfma_f32_16x16x32_bf16 v[172:175], a[232:235], v[72:75], v[172:175]\n"
    "v_mfma_f32_16x16x32_bf16 v[172:175], a[236:239], v[76:79], v[172:175]\n"
    "v_mfma_f32_16x16x32_bf16 v[172:175], a[240:243], v[80:83], v[172:175]\n"
    "v_mfma_f32_16x16x32_bf16 v[172:175], a[244:247], v[84:87], v[172:175]\n"
    "v_mfma_f32_16x16x32_bf16 v[172:175], a[248:251], v[88:91], v[172:175]\n"
    "v_mfma_f32_16x16x32_bf16 v[172:175], a[252:255], v[92:95], v[172:175]\n"
    "s_nop 7\n"
    "v_cvt_pk_bf16_f32 v200, v168, v172\n"
    "global_store_dword v17, v200, s[52:53] sc0 sc1 nt\n"
    "v_cvt_pk_bf16_f32 v202, v169, v173\n"
    "global_store_dword v17, v202, s[54:55] sc0 sc1 nt\n"
    "v_cvt_pk_bf16_f32 v204, v170, v174\n"
    "global_store_dword v17, v204, s[56:57] sc0 sc1 nt\n"
    "v_cvt_pk_bf16_f32 v206, v171, v175\n"
    "global_store_dword v17, v206, s[58:59] sc0 sc1 nt\n"
    "s_endpgm\n"
    "POLL1_%=:\n"
    "global_load_dword v20, v18, s[14:15] sc1\n"
    "s_waitcnt vmcnt(0)\n"
    "v_cmp_eq_u32_e64 s[68:69], v20, v19\n"
    "v_and_b32 v22, 15, v20\n"
    "v_cmp_gt_u32_e64 s[70:71], 1, v22\n"
    "s_or_b32 s68, s68, s70\n"
    "s_and_b32 s68, s68, 15\n"
    "s_cbranch_scc1 POLL1_%=\n"
    "s_branch BACK1_%=\n"
    "POLL2_%=:\n"
    "global_load_dword v20, v18, s[14:15] sc1\n"
    "s_waitcnt vmcnt(0)\n"
    "v_cmp_eq_u32_e64 s[68:69], v20, v19\n"
    "v_and_b32 v22, 15, v20\n"
    "v_cmp_gt_u32_e64 s[70:71], 3, v22\n"
    "s_or_b32 s68, s68, s70\n"
    "s_and_b32 s68, s68, 15\n"
    "s_cbranch_scc1 POLL2_%=\n"
    "s_branch BACK2_%=\n"
    "END_%=:\n"
    :
    : "s"(__builtin_amdgcn_kernarg_segment_ptr()), "s"(blockIdx.x), "v"(threadIdx.x)
    : "v1", "v2", "v3", "v4", "v5", "v6", "v7", "v8", "v9", "v10", "v11", "v12", "v13", "v14", "v15", "v16", "v17", "v18", "v19", "v20", "v21", "v22", "v23", "v24", "v25", "v26", "v27", "v28", "v29", "v30", "v31", "v32", "v33", "v34", "v35", "v36", "v37", "v38", "v39", "v40", "v41", "v42", "v43", "v44", "v45", "v46", "v47", "v48", "v49", "v50", "v51", "v52", "v53", "v54", "v55", "v56", "v57", "v58", "v59", "v60", "v61", "v62", "v63", "v64", "v65", "v66", "v67", "v68", "v69", "v70", "v71", "v72", "v73", "v74", "v75", "v76", "v77", "v78", "v79", "v80", "v81", "v82", "v83", "v84", "v85", "v86", "v87", "v88", "v89", "v90", "v91", "v92", "v93", "v94", "v95", "v96", "v97", "v98", "v99", "v100", "v101", "v102", "v103", "v104", "v105", "v106", "v107", "v108", "v109", "v110", "v111", "v112", "v113", "v114", "v115", "v116", "v117", "v118", "v119", "v120", "v121", "v122", "v123", "v124", "v125", "v126", "v127", "v128", "v129", "v130", "v131", "v132", "v133", "v134", "v135", "v136", "v137", "v138", "v139", "v140", "v141", "v142", "v143", "v144", "v145", "v146", "v147", "v148", "v149", "v150", "v151", "v152", "v153", "v154", "v155", "v156", "v157", "v158", "v159", "v160", "v161", "v162", "v163", "v164", "v165", "v166", "v167", "v168", "v169", "v170", "v171", "v172", "v173", "v174", "v175", "v176", "v177", "v178", "v179", "v180", "v181", "v182", "v183", "v184", "v185", "v186", "v187", "v188", "v189", "v190", "v191", "v192", "v193", "v194", "v195", "v196", "v197", "v198", "v199", "v200", "v201", "v202", "v203", "v204", "v205", "v206", "v207", "v208", "v209", "v210", "v211", "v212", "v213", "v214", "v215", "v216", "v217", "v218", "v219", "v220", "v221", "v222", "v223", "v224", "v225", "v226", "v227", "v228", "v229", "v230", "v231", "v232", "v233", "v234", "v235", "v236", "v237", "v238", "v239", "v240", "v241", "v242", "v243", "v244", "v245", "v246", "v247", "v248", "v249", "v250", "v251", "v252", "v253", "v254", "v255", "a0", "a1", "a2", "a3", "a4", "a5", "a6", "a7", "a8", "a9", "a10", "a11", "a12", "a13", "a14", "a15", "a16", "a17", "a18", "a19", "a20", "a21", "a22", "a23", "a24", "a25", "a26", "a27", "a28", "a29", "a30", "a31", "a32", "a33", "a34", "a35", "a36", "a37", "a38", "a39", "a40", "a41", "a42", "a43", "a44", "a45", "a46", "a47", "a48", "a49", "a50", "a51", "a52", "a53", "a54", "a55", "a56", "a57", "a58", "a59", "a60", "a61", "a62", "a63", "a64", "a65", "a66", "a67", "a68", "a69", "a70", "a71", "a72", "a73", "a74", "a75", "a76", "a77", "a78", "a79", "a80", "a81", "a82", "a83", "a84", "a85", "a86", "a87", "a88", "a89", "a90", "a91", "a92", "a93", "a94", "a95", "a96", "a97", "a98", "a99", "a100", "a101", "a102", "a103", "a104", "a105", "a106", "a107", "a108", "a109", "a110", "a111", "a112", "a113", "a114", "a115", "a116", "a117", "a118", "a119", "a120", "a121", "a122", "a123", "a124", "a125", "a126", "a127", "a128", "a129", "a130", "a131", "a132", "a133", "a134", "a135", "a136", "a137", "a138", "a139", "a140", "a141", "a142", "a143", "a144", "a145", "a146", "a147", "a148", "a149", "a150", "a151", "a152", "a153", "a154", "a155", "a156", "a157", "a158", "a159", "a160", "a161", "a162", "a163", "a164", "a165", "a166", "a167", "a168", "a169", "a170", "a171", "a172", "a173", "a174", "a175", "a176", "a177", "a178", "a179", "a180", "a181", "a182", "a183", "a184", "a185", "a186", "a187", "a188", "a189", "a190", "a191", "a192", "a193", "a194", "a195", "a196", "a197", "a198", "a199", "a200", "a201", "a202", "a203", "a204", "a205", "a206", "a207", "a208", "a209", "a210", "a211", "a212", "a213", "a214", "a215", "a216", "a217", "a218", "a219", "a220", "a221", "a222", "a223", "a224", "a225", "a226", "a227", "a228", "a229", "a230", "a231", "a232", "a233", "a234", "a235", "a236", "a237", "a238", "a239", "a240", "a241", "a242", "a243", "a244", "a245", "a246", "a247", "a248", "a249", "a250", "a251", "a252", "a253", "a254", "a255", "s4", "s5", "s6", "s7", "s8", "s9", "s10", "s11", "s12", "s13", "s14", "s15", "s16", "s17", "s18", "s19", "s20", "s21", "s22", "s23", "s24", "s25", "s26", "s27", "s28", "s29", "s30", "s31", "s33", "s34", "s35", "s36", "s37", "s38", "s39", "s40", "s41", "s42", "s43", "s44", "s45", "s46", "s47", "s48", "s49", "s50", "s51", "s52", "s53", "s54", "s55", "s56", "s57", "s58", "s59", "s60", "s61", "s62", "s63", "s64", "s65", "s66", "s67", "s68", "s69", "s70", "s71", "s72", "s73", "s74", "s75", "s76", "s77", "s78", "s79", "s80", "s81", "s82", "s83", "s84", "s85", "s86", "s87", "s88", "s89", "s90", "s91", "s92", "s93", "s94", "s95", "s96", "s97", "s98", "s99", "memory", "scc", "vcc");
}

extern "C" void kernel_launch(void* const* d_in, const int* in_sizes, int n_in, void* d_out, int out_size, void* d_ws,
                              size_t ws_size, hipStream_t stream) {
  (void)hipFuncSetAttribute((const void*)w4a16_gemm, hipFuncAttributeMaxDynamicSharedMemorySize, 154112);
  w4a16_gemm<<<256, 256, 154112, stream>>>((const char*)d_in[0], (const char*)d_in[1], (const char*)d_in[2], (const char*)d_in[3],
                                        (char*)d_out, (char*)d_ws);
}
```
